```python
import torch

def _softmax(x, axis=-1):
    return torch.softmax(x, dim=axis)

def _gelu(x):
    return torch.nn.functional.gelu(x, approximate='tanh')

def _silu(x):
    return torch.nn.functional.silu(x)

def _sigmoid(x):
    return torch.sigmoid(x)

def _layernorm(x, axis=-1, eps=1e-05):
    m = x.mean(dim=axis, keepdim=True)
    v = x.var(dim=axis, keepdim=True, unbiased=False)
    return (x - m) / torch.sqrt(v + eps)

def _scatter_add(tgt, idx, src, axis=0):
    return tgt.clone().index_add_(axis, idx, src)

def _segment_sum(src, seg, n):
    out = torch.zeros((n,) + src.shape[1:], dtype=src.dtype)
    return out.index_add_(0, seg, src)

def _pad(x, pw):
    flat = tuple((v for pair in reversed(pw) for v in pair))
    return torch.nn.functional.pad(x, flat)
kernel_name = 'adaLN_cond__sliding_block__glu_gated_geglu__prompt'

def setup_inputs():

    _w = lambda a: (lambda t: t.to(torch.bfloat16) if t.is_floating_point() else t)(torch.as_tensor(a))

    def _A_setup():
        B, S, D = (1, 2048, 448)
        return {'x': _w((lambda *s: torch.randn(*s, dtype=torch.bfloat16) * 0.05)(B, S, D)), 'wqkv': _w((lambda *s: torch.randn(*s, dtype=torch.bfloat16) * 0.05)(D, 3 * D)), 'wo': _w((lambda *s: torch.randn(*s, dtype=torch.bfloat16) * 0.05)(D, D))}
    _A_in = _A_setup()

    def _B_setup():
        B, S, Dm, Df = (1, 2048, 448, 1792)
        return {'x': _w((lambda *s: torch.randn(*s, dtype=torch.bfloat16) * 0.05)(B, S, Dm)), 'w_gate': _w((lambda *s: torch.randn(*s, dtype=torch.bfloat16) * 0.8)(Dm, Df) * Dm ** (-0.5)), 'w_up': _w((lambda *s: torch.randn(*s, dtype=torch.bfloat16) * 0.8)(Dm, Df) * Dm ** (-0.5)), 'w_down': _w((lambda *s: torch.randn(*s, dtype=torch.bfloat16) * 0.8)(Df, Dm) * Df ** (-0.5))}
    _B_in = _B_setup()
    return {'A_x': _A_in['x'], 'A_wqkv': _A_in['wqkv'], 'A_wo': _A_in['wo'], 'B_x': _B_in['x'], 'B_w_gate': _B_in['w_gate'], 'B_w_up': _B_in['w_up'], 'B_w_down': _B_in['w_down'], 'c': _w((lambda *s: torch.randn(*s, dtype=torch.bfloat16) * 0.1)(1, 448) * 8.0), 'w_mod': _w((lambda *s: torch.randn(*s, dtype=torch.bfloat16) * 0.1)(448, 6 * 448) * 448 ** (-0.5) * 8.0)}

def reference(A_x, A_wqkv, A_wo, B_x, B_w_gate, B_w_up, B_w_down, c, w_mod):

    def _A_ref(x, wqkv, wo):
        B, S, D = x.shape
        H = 16
        Dh = D // H
        W = 256
        qkv = (lambda x, *d: x.permute(*d))((x @ wqkv).reshape(B, S, 3, H, Dh), 2, 0, 3, 1, 4)
        q, k, v = (qkv[0], qkv[1], qkv[2])
        scores = torch.einsum('bhqd,bhkd->bhqk', q, k) * Dh ** (-0.5)
        idx = torch.arange(S)
        mask = (idx[:, None] - idx[None, :] >= 0) & (idx[:, None] - idx[None, :] < W)
        scores = torch.where(mask[None, None], scores, -1000000000.0)
        p = _softmax(scores, axis=-1)
        o = torch.einsum('bhqk,bhkd->bhqd', p, v)
        return (lambda x, *d: x.permute(*d))(o, 0, 2, 1, 3).reshape(B, S, D) @ wo

    def _B_ref(x, w_gate, w_up, w_down):
        g = torch.einsum('bsd,df->bsf', x, w_gate)
        u = torch.einsum('bsd,df->bsf', x, w_up)
        h = _gelu(g) * u
        return torch.einsum('bsf,fd->bsd', h, w_down)
    x = A_x
    mod = (_silu(c) @ w_mod).reshape(c.shape[0], 6, 1, -1)
    sa, ca, ga, sb, cb, gb = (mod[:, 0].clone(), mod[:, 1].clone(), mod[:, 2].clone(), mod[:, 3].clone(), mod[:, 4].clone(), mod[:, 5].clone())
    h = _layernorm(x, axis=-1) * (1 + ca) + sa
    x = x + ga * _A_ref(h, A_wqkv, A_wo)
    h = _layernorm(x, axis=-1) * (1 + cb) + sb
    x = x + gb * _B_ref(h, B_w_gate, B_w_up, B_w_down)
    return x
```

```cpp
#include <hip/hip_runtime.h>
#include <hip/hip_bf16.h>
#include <stdint.h>


typedef __attribute__((ext_vector_type(8))) __bf16 bf16x8;
typedef __attribute__((ext_vector_type(2))) __bf16 bf16x2;
typedef __attribute__((ext_vector_type(2))) _Float16 f16x2;
typedef __attribute__((ext_vector_type(4))) float f32x4;
typedef __attribute__((ext_vector_type(2))) float f32x2;
typedef __attribute__((ext_vector_type(4))) short s16x4;
typedef __attribute__((ext_vector_type(4))) unsigned u32x4;
typedef __attribute__((ext_vector_type(2))) unsigned u32x2;
typedef __attribute__((address_space(3))) char lds_char;
typedef unsigned short u16;

#define DI __device__ __forceinline__

constexpr size_t WS_MOD = 0;
constexpr size_t WS_U = 16384;
constexpr size_t WS_XN = 65536;
constexpr size_t WS_Q = WS_XN + 128 * 14 * 1024;
constexpr size_t WS_K = WS_Q + 16 * 128 * 1024;
constexpr size_t WS_V = WS_K + 16 * 128 * 1024;
constexpr size_t WS_O = WS_V + 16 * 128 * 1024;
constexpr size_t WS_X1 = WS_O + 128 * 14 * 1024;
constexpr size_t WS_XM = WS_X1 + 128 * 14 * 1024;
constexpr size_t WS_ST = WS_XM + 128 * 14 * 1024;
constexpr size_t WS_HH = WS_ST + 2048 * 7 * 8;

template <int N> struct IC { static constexpr int v = N; };
template <int I, int N, class F> DI void sfor(F&& f) {
  if constexpr (I < N) { f(IC<I>{}); sfor<I + 1, N>(f); }
}
template <int N> DI void wait_vm_bar() { asm volatile("s_waitcnt vmcnt(%0)\n\ts_barrier" ::"i"(N) : "memory"); }
DI void bar() { asm volatile("s_barrier" ::: "memory"); }
DI void sched_fence() { __builtin_amdgcn_sched_barrier(0); }

DI float bf_lo(unsigned u) { return __uint_as_float(u << 16); }
DI float bf_hi(unsigned u) { return __uint_as_float(u & 0xffff0000u); }
DI unsigned pk_bf16(float lo, float hi) {
  return __builtin_bit_cast(unsigned, __builtin_convertvector(f32x2{lo, hi}, bf16x2));
}
DI unsigned pk_f16(float lo, float hi) {
  return __builtin_bit_cast(unsigned, __builtin_convertvector(f32x2{lo, hi}, f16x2));
}
DI float h_lo(unsigned u) { return (float)__builtin_bit_cast(f16x2, u)[0]; }
DI float h_hi(unsigned u) { return (float)__builtin_bit_cast(f16x2, u)[1]; }
DI float bf_round(float x) { return bf_lo(pk_bf16(x, 0.f)); }
DI float bf2f(u16 v) { return __uint_as_float(((unsigned)v) << 16); }

DI void glds(const void* sbase, unsigned voff, unsigned laddr) {
  asm volatile("s_mov_b32 m0, %2\n\ts_nop 0\n\tglobal_load_lds_dwordx4 %0, %1" ::"v"(voff), "s"(sbase), "s"(laddr));
}
DI unsigned lds_addr(const lds_char* p) { return (unsigned)(uintptr_t)p; }
DI u32x4 lds_rd128(const lds_char* p) { return *(const __attribute__((address_space(3))) u32x4*)p; }
DI f32x4 lds_rd128f(const lds_char* p) { return *(const __attribute__((address_space(3))) f32x4*)p; }
DI u32x2 lds_tr(const lds_char* p) {
  return __builtin_bit_cast(u32x2, __builtin_amdgcn_ds_read_tr16_b64_v4i16((__attribute__((address_space(3))) s16x4*)p));
}
DI f32x4 mfma16(u32x4 a, u32x4 b, f32x4 c) {
  return __builtin_amdgcn_mfma_f32_16x16x32_bf16(__builtin_bit_cast(bf16x8, a), __builtin_bit_cast(bf16x8, b), c, 0, 0, 0);
}

__global__ __launch_bounds__(256) void k0_mod_ln(const u16* __restrict__ x, const u16* __restrict__ c,
                                                 const u16* __restrict__ wmod, char* __restrict__ ws) {
  __shared__ float red[64 * 33];
  const int tid = threadIdx.x;
  if (blockIdx.x < 84) {
    const int n0 = blockIdx.x * 32;
    const int cq = tid & 3, rg = tid >> 2;
    u32x4 w[7];
    float cv[7];
#pragma unroll
    for (int i = 0; i < 7; ++i) {
      w[i] = *(const u32x4*)(wmod + (size_t)(rg + 64 * i) * 2688 + n0 + cq * 8);
      cv[i] = bf2f(c[rg + 64 * i]);
    }
    float acc[8];
#pragma unroll
    for (int j = 0; j < 8; ++j) acc[j] = 0.f;
#pragma unroll
    for (int i = 0; i < 7; ++i) {
      const float s = bf_round(cv[i] / (1.f + __expf(-cv[i])));
#pragma unroll
      for (int j = 0; j < 4; ++j) {
        acc[2 * j] += s * bf_lo(w[i][j]);
        acc[2 * j + 1] += s * bf_hi(w[i][j]);
      }
    }
#pragma unroll
    for (int j = 0; j < 8; ++j) red[rg * 33 + cq * 8 + j] = acc[j];
    __syncthreads();
    const int col = tid >> 3, part = tid & 7;
    float s = 0.f;
#pragma unroll
    for (int i = 0; i < 8; ++i) s += red[(part * 8 + i) * 33 + col];
    s += __shfl_xor(s, 1);
    s += __shfl_xor(s, 2);
    s += __shfl_xor(s, 4);
    if (part == 0) {
      const int n = n0 + col;
      float v = bf_round(s);
      const int sec = n / 448;
      if (sec == 1 || sec == 4) v = bf_round(1.f + v);
      ((float*)(ws + WS_MOD))[n] = v;
    }
  } else {
    const int lane = tid & 63, wid = tid >> 6;
    const int row = (blockIdx.x - 84) * 32 + wid * 8 + (lane >> 3);
    const int i8 = lane & 7;
    u32x4 v[7];
#pragma unroll
    for (int m = 0; m < 7; ++m) v[m] = *(const u32x4*)(x + (size_t)row * 448 + (i8 + 8 * m) * 8);
    float s = 0.f;
#pragma unroll
    for (int m = 0; m < 7; ++m)
#pragma unroll
      for (int j = 0; j < 4; ++j) s += bf_lo(v[m][j]) + bf_hi(v[m][j]);
    s += __shfl_xor(s, 1);
    s += __shfl_xor(s, 2);
    s += __shfl_xor(s, 4);
    const float mean = s * (1.f / 448.f);
    float q = 0.f;
#pragma unroll
    for (int m = 0; m < 7; ++m)
#pragma unroll
      for (int j = 0; j < 4; ++j) {
        const float a = bf_lo(v[m][j]) - mean, b = bf_hi(v[m][j]) - mean;
        q += a * a + b * b;
      }
    q += __shfl_xor(q, 1);
    q += __shfl_xor(q, 2);
    q += __shfl_xor(q, 4);
    const float rstd = rsqrtf(q * (1.f / 448.f) + 1e-5f);
    char* dst = ws + WS_XN + (size_t)(row >> 4) * 14 * 1024 + (row & 15) * 16;
#pragma unroll
    for (int m = 0; m < 7; ++m) {
      u32x4 o;
#pragma unroll
      for (int j = 0; j < 4; ++j) o[j] = pk_f16((bf_lo(v[m][j]) - mean) * rstd, (bf_hi(v[m][j]) - mean) * rstd);
      const int ch = i8 + 8 * m;
      *(u32x4*)(dst + (ch >> 2) * 1024 + (ch & 3) * 256) = o;
    }
  }
}

template <int RB, int NSEG, int NBS, int KS, int SK, int NSLOT, int LEAD, int LDW, bool PERM, bool SWAP>
struct Gemm {
  static constexpr int NST = KS / SK;
  static constexpr int RS = 32 * NBS;
  static constexpr int PPK = 4 * RB + NSEG * NBS;
  static constexpr int NP = SK * PPK;
  static constexpr int STG = NP * 1024;
  static constexpr int LDS_BYTES = NSLOT * STG;
  static constexpr int NG = NSEG * NBS;
  static_assert(LEAD <= SK && NSLOT >= 3, "cfg");

  template <int WL>
  static DI void load(unsigned lds0, const char* Ablk, const u16* W0, const u16* W1, int n0, int lane) {
    constexpr int CNT = (NP - WL + 3) / 4;
    unsigned voff[CNT];
    sfor<0, CNT>([&](auto ci) {
      constexpr int i = WL + 4 * decltype(ci)::v;
      constexpr int k = i / PPK, j = i % PPK;
      if constexpr (j < 4 * RB) {
        voff[decltype(ci)::v] = (j * KS + k) * 1024 + lane * 16;
      } else {
        constexpr int pb = (j - 4 * RB) % NBS;
        int pos, col;
        if constexpr (NBS == 7) {
          const int cidx = pb * 64 + lane;
          pos = (cidx * 4682) >> 16;
          col = (cidx - pos * 14) * 8;
        } else {
          pos = 8 * pb + (lane >> 3);
          const int slot = lane & 7;
          col = 16 * ((slot >> 1) ^ ((pos >> 1) & 3)) + 8 * (slot & 1);
        }
        const int kk = PERM ? (8 * ((pos >> 2) & 3) + 4 * (pos >> 4) + (pos & 3)) : pos;
        voff[decltype(ci)::v] = ((k * 32 + kk) * LDW + n0 + col) * 2;
      }
    });
    auto issue = [&](auto tt) {
      constexpr int T = decltype(tt)::v;
      sfor<0, CNT>([&](auto ci) {
        constexpr int i = WL + 4 * decltype(ci)::v;
        constexpr int j = i % PPK;
        const char* sb;
        if constexpr (j < 4 * RB) sb = Ablk + (size_t)T * SK * 1024;
        else if constexpr ((j - 4 * RB) / NBS == 0) sb = (const char*)W0 + (size_t)T * SK * 32 * LDW * 2;
        else sb = (const char*)W1 + (size_t)T * SK * 32 * LDW * 2;
        glds(sb, voff[decltype(ci)::v], lds0 + (T % NSLOT) * STG + i * 1024);
      });
    };
    sfor<0, (NSLOT < NST ? NSLOT : NST)>(issue);
    sfor<0, NST>([&](auto tt) {
      constexpr int T = decltype(tt)::v;
      constexpr int l0 = (T - 3 + NSLOT > NSLOT - 1) ? (T - 3 + NSLOT) : (NSLOT - 1);
      constexpr int last = l0 < NST - 1 ? l0 : NST - 1;
      wait_vm_bar<(last - T) * CNT>();
      if constexpr (T >= 2 && T - 2 + NSLOT < NST) issue(IC<T - 2 + NSLOT>{});
    });
  }
  static DI void load_any(int wl, unsigned lds0, const char* Ablk, const u16* W0, const u16* W1, int n0, int lane) {
    if (wl == 0) load<0>(lds0, Ablk, W0, W1, n0, lane);
    else if (wl == 1) load<1>(lds0, Ablk, W0, W1, n0, lane);
    else if (wl == 2) load<2>(lds0, Ablk, W0, W1, n0, lane);
    else load<3>(lds0, Ablk, W0, W1, n0, lane);
  }

  template <class XR, class XF>
  static DI void compute(const lds_char* lds, f32x4 (&acc)[RB][NG], int wid, int lane, XR&& xr, XF&& xf) {
    const lds_char* ap = lds + wid * RB * 1024 + lane * 16;
    const lds_char* bp;
    if constexpr (NBS == 7) bp = lds + 4 * RB * 1024 + (lane >> 2) * 224 + 8 * (lane & 3);
    else bp = lds + 4 * RB * 1024;
    const unsigned tb = (lane >> 2) * 128 + (((lane >> 3) & 3) << 5) + 8 * (lane & 3);
    u32x4 a[LEAD + 1][RB];
    u32x4 b[LEAD][NG];
    auto rdA = [&](auto ss) {
      constexpr int s = decltype(ss)::v;
      constexpr int off = ((s / SK) % NSLOT) * STG + (s % SK) * PPK * 1024;
#pragma unroll
      for (int rb = 0; rb < RB; ++rb) a[s % (LEAD + 1)][rb] = lds_rd128(ap + off + rb * 1024);
    };
    auto rdB = [&](auto ss, auto gg) {
      constexpr int s = decltype(ss)::v, g = decltype(gg)::v;
      constexpr int off = ((s / SK) % NSLOT) * STG + (s % SK) * PPK * 1024 + (g / NBS) * NBS * 1024;
      const lds_char* p;
      if constexpr (NBS == 7) p = bp + off + (g % NBS) * 32;
      else p = bp + off + (tb ^ ((g % NBS) << 5));
      const u32x2 b0 = lds_tr(p), b1 = lds_tr(p + 16 * RS);
      b[s % LEAD][g] = u32x4{b0[0], b0[1], b1[0], b1[1]};
    };
    bar();
    sfor<0, LEAD>([&](auto ss) {
      rdA(ss);
      xr(ss, a[decltype(ss)::v % (LEAD + 1)]);
      sfor<0, NG>([&](auto gg) { rdB(ss, gg); });
    });
    sfor<0, NG>([&](auto gg) { xf(IC<0>{}, gg, a[0]); });
    sched_fence();
    sfor<0, KS>([&](auto ss) {
      constexpr int s = decltype(ss)::v;
      constexpr int sn = s + LEAD;
      if constexpr (sn < KS) {
        if constexpr (sn % SK == 0) bar();
        rdA(IC<sn>{});
        xr(IC<sn>{}, a[sn % (LEAD + 1)]);
      }
      sfor<0, NG>([&](auto gg) {
        constexpr int g = decltype(gg)::v;
#pragma unroll
        for (int rb = 0; rb < RB; ++rb)
          acc[rb][g] = SWAP ? mfma16(b[s % LEAD][g], a[s % (LEAD + 1)][rb], acc[rb][g])
                            : mfma16(a[s % (LEAD + 1)][rb], b[s % LEAD][g], acc[rb][g]);
        if constexpr (sn < KS) rdB(IC<sn>{}, gg);
        if constexpr (s + 1 < KS) xf(IC<s + 1>{}, gg, a[(s + 1) % (LEAD + 1)]);
        sched_fence();
      });
    });
  }
};

struct NoX {
  template <class A, class B> DI void operator()(A, B&) const {}
  template <class A, class B, class C> DI void operator()(A, B, C&) const {}
};

typedef Gemm<2, 1, 7, 14, 1, 9, 1, 1344, true, true> G1S;
typedef Gemm<2, 1, 7, 14, 1, 9, 1, 1344, true, false> G1N;

template <bool SWAP>
DI void k1_compute(const lds_char* lds, char* __restrict__ ws, int mt, int nt, int wid, int lane) {
  typedef Gemm<2, 1, 7, 14, 1, 9, 1, 1344, true, SWAP> G;
  const lds_char* vec = lds + G::LDS_BYTES;
  f32x4 acc[2][7];
#pragma unroll
  for (int rb = 0; rb < 2; ++rb)
#pragma unroll
    for (int nb = 0; nb < 7; ++nb) acc[rb][nb] = f32x4{0.f, 0.f, 0.f, 0.f};
  const int gq = lane >> 4, t = lane & 15;
  const lds_char* vp = vec + gq * 32;
  f32x4 sv[2], mv[2];
  G::compute(
      lds, acc, wid, lane,
      [&](auto ss, u32x4(&)[2]) {
        constexpr int s = decltype(ss)::v;
        sv[0] = lds_rd128f(vp + s * 128);
        sv[1] = lds_rd128f(vp + s * 128 + 16);
        mv[0] = lds_rd128f(vp + 1792 + s * 128);
        mv[1] = lds_rd128f(vp + 1792 + s * 128 + 16);
      },
      [&](auto, auto gg, u32x4(&a)[2]) {
        constexpr int g = decltype(gg)::v;
        if constexpr (g >= 3) {
          constexpr int j = g - 3;
#pragma unroll
          for (int rb = 0; rb < 2; ++rb)
            a[rb][j] = pk_bf16(h_lo(a[rb][j]) * mv[j >> 1][(j & 1) * 2] + sv[j >> 1][(j & 1) * 2],
                               h_hi(a[rb][j]) * mv[j >> 1][(j & 1) * 2 + 1] + sv[j >> 1][(j & 1) * 2 + 1]);
        }
      });
  const int sec = nt >> 2, h0 = (nt & 3) * 4;
  if constexpr (SWAP) {
    const float sc = sec == 0 ? 0.18898223650461362f * 1.4426950408889634f : 1.f;
    char* base = ws + (sec == 0 ? WS_Q : WS_K);
#pragma unroll
    for (int rb = 0; rb < 2; ++rb) {
      const int tb = mt * 8 + wid * 2 + rb;
#pragma unroll
      for (int nb = 0; nb < 7; ++nb) {
        const int Gi = 4 * nb + gq;
        const int hd = (Gi * 37) >> 8, dg = Gi - 7 * hd;
        char* dst = base + ((size_t)(h0 + hd) * 128 + tb) * 1024 + ((dg >> 1) * 16 + t) * 16 + (dg & 1) * 8;
        const f32x4 v = acc[rb][nb];
        const unsigned lo = pk_bf16(v[0] * sc, v[1] * sc), hi = pk_bf16(v[2] * sc, v[3] * sc);
        if (dg == 6) *(u32x4*)dst = u32x4{lo, hi, 0u, 0u};
        else *(u32x2*)dst = u32x2{lo, hi};
      }
    }
  } else {
    const int s32 = mt * 4 + wid;
    char* base = ws + WS_V;
#pragma unroll
    for (int nb = 0; nb < 7; ++nb) {
      const int n = 16 * nb + t;
      const int hd = (n * 147) >> 12, d = n - 28 * hd;
      char* dst = base + (((size_t)(h0 + hd) * 64 + s32) * 2 + (d >> 4)) * 1024 + (gq * 16 + (d & 15)) * 16;
      const f32x4 v0 = acc[0][nb], v1 = acc[1][nb];
      *(u32x4*)dst = u32x4{pk_bf16(v0[0], v0[1]), pk_bf16(v0[2], v0[3]), pk_bf16(v1[0], v1[1]), pk_bf16(v1[2], v1[3])};
    }
    if (lane < 16) {
      char* dst = base + (((size_t)(h0 + (lane >> 2)) * 64 + s32) * 2 + 1) * 1024 + ((lane & 3) * 16 + 12) * 16;
      *(u32x4*)dst = u32x4{0x3f803f80u, 0x3f803f80u, 0x3f803f80u, 0x3f803f80u};
    }
  }
}

__global__ __launch_bounds__(512, 1) void k1_qkv(const u16* __restrict__ wqkv, const u16* __restrict__ wg,
                                                 const u16* __restrict__ wu, char* __restrict__ ws) {
  extern __shared__ __attribute__((aligned(16))) char smem[];
  lds_char* lds = (lds_char*)smem;
  const int lane = threadIdx.x & 63, wid = __builtin_amdgcn_readfirstlane(threadIdx.x >> 6);
  const int bid = blockIdx.x, xcd = bid & 7, j = bid >> 3;
  if (j >= 24) {
    const int e = (j - 24) * 8 + xcd;
    const int mat = e >> 5, c0 = (e & 31) * 56;
    const u16* W = mat ? wu : wg;
    const int tid = threadIdx.x;
    float* red = (float*)smem;
    if (tid < 448) {
      const int rg = tid / 7, cq = tid - rg * 7;
      u32x4 w[7];
      float m[7], sb[7];
#pragma unroll
      for (int i = 0; i < 7; ++i) {
        const int k = rg + 64 * i;
        w[i] = *(const u32x4*)(W + (size_t)k * 1792 + c0 + cq * 8);
        sb[i] = ((const float*)(ws + WS_MOD))[3 * 448 + k];
        m[i] = ((const float*)(ws + WS_MOD))[4 * 448 + k];
      }
      float a1[8], a2[8];
#pragma unroll
      for (int q = 0; q < 8; ++q) a1[q] = a2[q] = 0.f;
#pragma unroll
      for (int i = 0; i < 7; ++i)
#pragma unroll
        for (int q = 0; q < 4; ++q) {
          a1[2 * q] += m[i] * bf_lo(w[i][q]);
          a1[2 * q + 1] += m[i] * bf_hi(w[i][q]);
          a2[2 * q] += sb[i] * bf_lo(w[i][q]);
          a2[2 * q + 1] += sb[i] * bf_hi(w[i][q]);
        }
#pragma unroll
      for (int q = 0; q < 8; ++q) {
        red[rg * 113 + cq * 8 + q] = a1[q];
        red[rg * 113 + 56 + cq * 8 + q] = a2[q];
      }
    }
    __syncthreads();
    if (tid < 448) {
      const int col = tid >> 2, part = tid & 3;
      float s = 0.f;
#pragma unroll
      for (int i = 0; i < 16; ++i) s += red[(part * 16 + i) * 113 + col];
      s += __shfl_xor(s, 1);
      s += __shfl_xor(s, 2);
      if (part == 0) {
        const int which = col >= 56, n = c0 + col - which * 56;
        const int nt = n / 112;
        ((float*)(ws + WS_U))[(nt * 4 + mat * 2 + which) * 112 + n - nt * 112] = s;
      }
    }
    return;
  }
  const int mt = (xcd >> 1) * 4 + (j & 3), nt = (xcd & 1) * 6 + (j >> 2);
  if (wid >= 4) {
    const int off = (wid - 4) * 1024 + lane * 16;
    glds(ws + WS_MOD, off < 3584 ? off : 0, lds_addr(lds) + G1S::LDS_BYTES + (wid - 4) * 1024);
    G1S::load_any(wid - 4, lds_addr(lds), ws + WS_XN + (size_t)mt * 8 * 14 * 1024, wqkv, wqkv, nt * 112, lane);
  } else {
    if (nt < 8) k1_compute<true>(lds, ws, mt, nt, wid, lane);
    else k1_compute<false>(lds, ws, mt, nt, wid, lane);
  }
}

template <int ODD, bool EDGE>
DI void k2_wave(const lds_char* lds, char* __restrict__ ws, int qt, int head, int wid, int lane) {
  const int gq = lane >> 4, t = lane & 15;
  const int lp0 = wid >> 1;
  const int qb = 8 * qt + wid;
  const lds_char* kp = lds + lp0 * 2048 + lane * 16;
  const lds_char* vp = lds + 24576 + lp0 * 2048 + lane * 16;
  const u32x4 qf = lds_rd128(lds + 49152 + wid * 1024 + lane * 16);
  f32x4 sc[17];
  const float NEG = -__builtin_inff();
#pragma unroll
  for (int i = 0; i < 17; ++i) {
    const u32x4 kf = lds_rd128(kp + (i + ODD) * 1024);
    sc[i] = mfma16(kf, qf, f32x4{0.f, 0.f, 0.f, 0.f});
  }
#pragma unroll
  for (int r = 0; r < 4; ++r) {
    if (!(t < 4 * gq + r)) sc[0][r] = NEG;
    if (t < 4 * gq + r) sc[16][r] = NEG;
  }
  if constexpr (EDGE) {
#pragma unroll
    for (int i = 0; i < 16; ++i)
      if (qb - 16 + i < 0) sc[i] = f32x4{NEG, NEG, NEG, NEG};
  }
  float m = sc[16][0];
#pragma unroll
  for (int i = 0; i < 17; ++i) m = fmaxf(fmaxf(m, fmaxf(sc[i][0], sc[i][1])), fmaxf(sc[i][2], sc[i][3]));
  m = fmaxf(m, __shfl_xor(m, 16));
  m = fmaxf(m, __shfl_xor(m, 32));
  unsigned pp[18][2];
#pragma unroll
  for (int i = 0; i < 17; ++i) {
    pp[i + ODD][0] = pk_bf16(__builtin_amdgcn_exp2f(sc[i][0] - m), __builtin_amdgcn_exp2f(sc[i][1] - m));
    pp[i + ODD][1] = pk_bf16(__builtin_amdgcn_exp2f(sc[i][2] - m), __builtin_amdgcn_exp2f(sc[i][3] - m));
  }
  pp[ODD ? 0 : 17][0] = 0u;
  pp[ODD ? 0 : 17][1] = 0u;
  wait_vm_bar<0>();
  f32x4 o0 = {0.f, 0.f, 0.f, 0.f}, o1 = {0.f, 0.f, 0.f, 0.f};
#pragma unroll
  for (int p = 0; p < 9; ++p) {
    const u32x4 pf = {pp[2 * p][0], pp[2 * p][1], pp[2 * p + 1][0], pp[2 * p + 1][1]};
    const u32x4 v0 = lds_rd128(vp + p * 2048), v1 = lds_rd128(vp + p * 2048 + 1024);
    o0 = mfma16(v0, pf, o0);
    o1 = mfma16(v1, pf, o1);
  }
  const float sum = __shfl(o1[0], 48 + t);
  const float inv = 1.f / sum;
  char* base = ws + WS_O + (size_t)qb * 14 * 1024;
  {
    const int Gi = 7 * head + gq;
    *(u32x2*)(base + (Gi >> 3) * 1024 + (((Gi >> 1) & 3) * 16 + t) * 16 + (Gi & 1) * 8) =
        u32x2{pk_bf16(o0[0] * inv, o0[1] * inv), pk_bf16(o0[2] * inv, o0[3] * inv)};
  }
  if (gq < 3) {
    const int Gi = 7 * head + 4 + gq;
    *(u32x2*)(base + (Gi >> 3) * 1024 + (((Gi >> 1) & 3) * 16 + t) * 16 + (Gi & 1) * 8) =
        u32x2{pk_bf16(o1[0] * inv, o1[1] * inv), pk_bf16(o1[2] * inv, o1[3] * inv)};
  }
}

__global__ __launch_bounds__(512, 2) void k2_attn(char* __restrict__ ws) {
  extern __shared__ __attribute__((aligned(16))) char smem[];
  lds_char* lds = (lds_char*)smem;
  const int lane = threadIdx.x & 63, wid = __builtin_amdgcn_readfirstlane(threadIdx.x >> 6);
  const int bid = blockIdx.x, xcd = bid & 7, j = bid >> 3;
  const int head = xcd * 2 + (j & 1), qt = j >> 1;
  const unsigned l0 = lds_addr(lds);
  glds(ws + WS_Q + ((size_t)head * 128 + 8 * qt + wid) * 1024, lane * 16, l0 + 49152 + wid * 1024);
#pragma unroll
  for (int i = 0; i < 3; ++i) {
    const int lk = wid * 3 + i;
    int tb = 8 * qt - 16 + lk;
    tb = tb < 0 ? 0 : tb;
    glds(ws + WS_K + ((size_t)head * 128 + tb) * 1024, lane * 16, l0 + lk * 1024);
  }
#pragma unroll
  for (int i = 0; i < 3; ++i) {
    const int lv = wid * 3 + i;
    int f = (4 * qt - 8) * 2 + lv;
    f = f < 0 ? (lv & 1) : f;
    glds(ws + WS_V + ((size_t)head * 128 + f) * 1024, lane * 16, l0 + 24576 + lv * 1024);
  }
  wait_vm_bar<3>();
  if (qt >= 2) {
    if (wid & 1) k2_wave<1, false>(lds, ws, qt, head, wid, lane);
    else k2_wave<0, false>(lds, ws, qt, head, wid, lane);
  } else {
    if (wid & 1) k2_wave<1, true>(lds, ws, qt, head, wid, lane);
    else k2_wave<0, true>(lds, ws, qt, head, wid, lane);
  }
}

typedef Gemm<1, 1, 4, 14, 2, 7, 2, 448, true, true> G3;
__global__ __launch_bounds__(512, 1) void k3_wo(const u16* __restrict__ x, const u16* __restrict__ wo, char* __restrict__ ws) {
  extern __shared__ __attribute__((aligned(16))) char smem[];
  lds_char* lds = (lds_char*)smem;
  const int lane = threadIdx.x & 63, wid = __builtin_amdgcn_readfirstlane(threadIdx.x >> 6);
  const int bid = blockIdx.x, xcd = bid & 7, j = bid >> 3;
  const int mt = xcd * 4 + (j & 3), nt = j >> 2;
  if (wid >= 4) {
    G3::load_any(wid - 4, lds_addr(lds), ws + WS_O + (size_t)mt * 4 * 14 * 1024, wo, wo, nt * 64, lane);
    return;
  }
  const int gq = lane >> 4, t = lane & 15;
  const int tb = mt * 4 + wid, row = tb * 16 + t;
  u32x2 xr[4];
  f32x4 ga[4], m2[4];
#pragma unroll
  for (int nb = 0; nb < 4; ++nb) {
    xr[nb] = *(const u32x2*)(x + (size_t)row * 448 + nt * 64 + nb * 16 + gq * 4);
    ga[nb] = *(const f32x4*)((const float*)(ws + WS_MOD) + 2 * 448 + nt * 64 + nb * 16 + gq * 4);
    m2[nb] = *(const f32x4*)((const float*)(ws + WS_MOD) + 4 * 448 + nt * 64 + nb * 16 + gq * 4);
  }
  f32x4 acc[1][4];
#pragma unroll
  for (int nb = 0; nb < 4; ++nb) acc[0][nb] = f32x4{0.f, 0.f, 0.f, 0.f};
  G3::compute(lds, acc, wid, lane, NoX{}, NoX{});
  unsigned o[4][2], om[4][2];
  float v[4][4];
  float s = 0.f;
#pragma unroll
  for (int nb = 0; nb < 4; ++nb) {
    o[nb][0] = pk_bf16(bf_lo(xr[nb][0]) + ga[nb][0] * acc[0][nb][0], bf_hi(xr[nb][0]) + ga[nb][1] * acc[0][nb][1]);
    o[nb][1] = pk_bf16(bf_lo(xr[nb][1]) + ga[nb][2] * acc[0][nb][2], bf_hi(xr[nb][1]) + ga[nb][3] * acc[0][nb][3]);
    v[nb][0] = bf_lo(o[nb][0]); v[nb][1] = bf_hi(o[nb][0]); v[nb][2] = bf_lo(o[nb][1]); v[nb][3] = bf_hi(o[nb][1]);
    om[nb][0] = pk_bf16(v[nb][0] * m2[nb][0], v[nb][1] * m2[nb][1]);
    om[nb][1] = pk_bf16(v[nb][2] * m2[nb][2], v[nb][3] * m2[nb][3]);
    s += (v[nb][0] + v[nb][1]) + (v[nb][2] + v[nb][3]);
  }
  char* dst = ws + WS_X1 + ((size_t)tb * 14 + nt * 2) * 1024 + lane * 16;
  *(u32x4*)(dst + (WS_XM - WS_X1)) = u32x4{om[0][0], om[0][1], om[1][0], om[1][1]};
  *(u32x4*)(dst + (WS_XM - WS_X1) + 1024) = u32x4{om[2][0], om[2][1], om[3][0], om[3][1]};
  *(u32x4*)dst = u32x4{o[0][0], o[0][1], o[1][0], o[1][1]};
  *(u32x4*)(dst + 1024) = u32x4{o[2][0], o[2][1], o[3][0], o[3][1]};
  s += __shfl_xor(s, 16);
  s += __shfl_xor(s, 32);
  const float mc = s * (1.f / 64.f);
  float q = 0.f;
#pragma unroll
  for (int nb = 0; nb < 4; ++nb)
#pragma unroll
    for (int r = 0; r < 4; ++r) q += (v[nb][r] - mc) * (v[nb][r] - mc);
  q += __shfl_xor(q, 16);
  q += __shfl_xor(q, 32);
  if (gq == 0) *(f32x2*)(ws + WS_ST + ((size_t)row * 7 + nt) * 8) = f32x2{mc, q};
}

DI float gelu_tanh(float x) {
  const float u = 0.7978845608028654f * (x + 0.044715f * x * x * x);
  return x / (1.f + __expf(-2.f * u));
}

typedef Gemm<2, 2, 7, 14, 1, 6, 1, 1792, false, true> G4;
__global__ __launch_bounds__(512, 1) void k4_gateup(const u16* __restrict__ wg, const u16* __restrict__ wu, char* __restrict__ ws) {
  extern __shared__ __attribute__((aligned(16))) char smem[];
  lds_char* lds = (lds_char*)smem;
  const int lane = threadIdx.x & 63, wid = __builtin_amdgcn_readfirstlane(threadIdx.x >> 6);
  const int bid = blockIdx.x, xcd = bid & 7, j = bid >> 3;
  const int mt = (xcd >> 2) * 8 + (j & 7), nt = (xcd & 3) * 4 + (j >> 3);
  if (wid >= 4) {
    const int wl = wid - 4;
    const unsigned ex = lds_addr(lds) + G4::LDS_BYTES;
    if (wl < 2) {
      const int off = wl * 1024 + lane * 16;
      glds(ws + WS_U + (size_t)nt * 1792, off < 1792 ? off : 0, ex + wl * 1024);
    }
#pragma unroll
    for (int i = 0; i < 2; ++i) {
      const int o2 = (wl * 2 + i) * 1024 + lane * 16;
      glds(ws + WS_ST + (size_t)mt * 7168, o2 < 7168 ? o2 : 0, ex + 2048 + (wl * 2 + i) * 1024);
    }
    G4::load_any(wl, lds_addr(lds), ws + WS_XM + (size_t)mt * 8 * 14 * 1024, wg, wu, nt * 112, lane);
    return;
  }
  const int gq = lane >> 4, t = lane & 15;
  f32x4 acc[2][14];
#pragma unroll
  for (int rb = 0; rb < 2; ++rb)
#pragma unroll
    for (int nb = 0; nb < 14; ++nb) acc[rb][nb] = f32x4{0.f, 0.f, 0.f, 0.f};
  G4::compute(lds, acc, wid, lane, NoX{}, NoX{});
  const lds_char* ex = lds + G4::LDS_BYTES;
#pragma unroll
  for (int rb = 0; rb < 2; ++rb) {
    const lds_char* sp = ex + 2048 + ((wid * 2 + rb) * 16 + t) * 56;
    float mc[7], q = 0.f, sm = 0.f;
#pragma unroll
    for (int c = 0; c < 7; ++c) {
      const f32x2 e = *(const __attribute__((address_space(3))) f32x2*)(sp + c * 8);
      mc[c] = e[0]; q += e[1]; sm += e[0];
    }
    const float mean = sm * (1.f / 7.f);
#pragma unroll
    for (int c = 0; c < 7; ++c) q += 64.f * (mc[c] - mean) * (mc[c] - mean);
    const float rs = rsqrtf(q * (1.f / 448.f) + 1e-5f);
    const int tb = mt * 8 + wid * 2 + rb;
    char* base = ws + WS_HH + (size_t)tb * 56 * 1024 + lane * 16;
#pragma unroll
    for (int nb = 0; nb < 7; ++nb) {
      const lds_char* up = ex + (nb * 16 + gq * 4) * 4;
      const f32x4 u1g = lds_rd128f(up), u2g = lds_rd128f(up + 448), u1u = lds_rd128f(up + 896), u2u = lds_rd128f(up + 1344);
      float h[4];
#pragma unroll
      for (int r = 0; r < 4; ++r) {
        const float gv = rs * (acc[rb][nb][r] - mean * u1g[r]) + u2g[r];
        const float uv = rs * (acc[rb][7 + nb][r] - mean * u1u[r]) + u2u[r];
        h[r] = gelu_tanh(gv) * uv;
      }
      const int B = nt * 7 + nb;
      *(u32x2*)(base + (B >> 1) * 1024 + (B & 1) * 8) = u32x2{pk_bf16(h[0], h[1]), pk_bf16(h[2], h[3])};
    }
  }
}

typedef Gemm<1, 1, 4, 56, 2, 9, 2, 448, false, true> G5;
__global__ __launch_bounds__(512, 1) void k5_down(const u16* __restrict__ wd, char* __restrict__ ws, u16* __restrict__ out) {
  extern __shared__ __attribute__((aligned(16))) char smem[];
  lds_char* lds = (lds_char*)smem;
  const int lane = threadIdx.x & 63, wid = __builtin_amdgcn_readfirstlane(threadIdx.x >> 6);
  const int bid = blockIdx.x, xcd = bid & 7, j = bid >> 3;
  const int mt = xcd * 4 + (j & 3), nt = j >> 2;
  if (wid >= 4) {
    G5::load_any(wid - 4, lds_addr(lds), ws + WS_HH + (size_t)mt * 4 * 56 * 1024, wd, wd, nt * 64, lane);
    return;
  }
  const int gq = lane >> 4, t = lane & 15;
  const int tb = mt * 4 + wid, row = tb * 16 + t;
  u32x4 xr[2];
  f32x4 gb[4];
#pragma unroll
  for (int i = 0; i < 2; ++i) xr[i] = *(const u32x4*)(ws + WS_X1 + ((size_t)tb * 14 + nt * 2 + i) * 1024 + lane * 16);
#pragma unroll
  for (int nb = 0; nb < 4; ++nb) gb[nb] = *(const f32x4*)((const float*)(ws + WS_MOD) + 5 * 448 + nt * 64 + nb * 16 + gq * 4);
  f32x4 acc[1][4];
#pragma unroll
  for (int nb = 0; nb < 4; ++nb) acc[0][nb] = f32x4{0.f, 0.f, 0.f, 0.f};
  G5::compute(lds, acc, wid, lane, NoX{}, NoX{});
#pragma unroll
  for (int nb = 0; nb < 4; ++nb) {
    const unsigned x0 = xr[nb >> 1][(nb & 1) * 2], x1 = xr[nb >> 1][(nb & 1) * 2 + 1];
    const unsigned lo = pk_bf16(bf_lo(x0) + gb[nb][0] * acc[0][nb][0], bf_hi(x0) + gb[nb][1] * acc[0][nb][1]);
    const unsigned hi = pk_bf16(bf_lo(x1) + gb[nb][2] * acc[0][nb][2], bf_hi(x1) + gb[nb][3] * acc[0][nb][3]);
    *(u32x2*)(out + (size_t)row * 448 + nt * 64 + nb * 16 + gq * 4) = u32x2{lo, hi};
  }
}

extern "C" void kernel_launch(void* const* d_in, const int* in_sizes, int n_in, void* d_out, int out_size, void* d_ws,
                              size_t ws_size, hipStream_t stream) {
  const u16* x = (const u16*)d_in[0];
  const u16* wqkv = (const u16*)d_in[1];
  const u16* wo = (const u16*)d_in[2];
  const u16* wg = (const u16*)d_in[4];
  const u16* wu = (const u16*)d_in[5];
  const u16* wd = (const u16*)d_in[6];
  const u16* c = (const u16*)d_in[7];
  const u16* wmod = (const u16*)d_in[8];
  char* ws = (char*)d_ws;
  constexpr int L1 = G1S::LDS_BYTES + 4096;
  constexpr int L2 = 49152 + 8192;
  constexpr int L3 = G3::LDS_BYTES;
  constexpr int L4 = G4::LDS_BYTES + 2048 + 8192;
  constexpr int L5 = G5::LDS_BYTES;
  static_assert(L1 <= 163840 && L3 <= 163840 && L4 <= 163840 && L5 <= 163840, "lds");
  (void)hipFuncSetAttribute((const void*)k1_qkv, hipFuncAttributeMaxDynamicSharedMemorySize, L1);
  (void)hipFuncSetAttribute((const void*)k3_wo, hipFuncAttributeMaxDynamicSharedMemorySize, L3);
  (void)hipFuncSetAttribute((const void*)k4_gateup, hipFuncAttributeMaxDynamicSharedMemorySize, L4);
  (void)hipFuncSetAttribute((const void*)k5_down, hipFuncAttributeMaxDynamicSharedMemorySize, L5);
  k0_mod_ln<<<148, 256, 0, stream>>>(x, c, wmod, ws);
  k1_qkv<<<256, 512, L1, stream>>>(wqkv, wg, wu, ws);
  k2_attn<<<256, 512, L2, stream>>>(ws);
  k3_wo<<<224, 512, L3, stream>>>(x, wo, ws);
  k4_gateup<<<256, 512, L4, stream>>>(wg, wu, ws);
  k5_down<<<224, 512, L5, stream>>>(wd, ws, (u16*)d_out);
}
```

```cpp
#include <hip/hip_runtime.h>
#include <hip/hip_bf16.h>
#include <stdint.h>

typedef __attribute__((ext_vector_type(8))) __bf16 bf16x8;
typedef __attribute__((ext_vector_type(2))) __bf16 bf16x2;
typedef __attribute__((ext_vector_type(2))) _Float16 f16x2;
typedef __attribute__((ext_vector_type(4))) float f32x4;
typedef __attribute__((ext_vector_type(2))) float f32x2;
typedef __attribute__((ext_vector_type(4))) short s16x4;
typedef __attribute__((ext_vector_type(4))) unsigned u32x4;
typedef __attribute__((ext_vector_type(2))) unsigned u32x2;
typedef __attribute__((address_space(3))) char lds_char;
typedef unsigned short u16;

#define DI __device__ __forceinline__
#define TDECL
#define TS(i)
#define TC(i)
#define TFLUSH(k)
#define TARG
#define TPASS

constexpr size_t WS_MOD = 0;
constexpr size_t WS_U = 16384;
constexpr size_t WS_XN = 65536;
constexpr size_t WS_Q = WS_XN + 128 * 14 * 1024;
constexpr size_t WS_K = WS_Q + 16 * 128 * 1024;
constexpr size_t WS_V = WS_K + 16 * 128 * 1024;
constexpr size_t WS_O = WS_V + 16 * 128 * 1024;
constexpr size_t WS_X1 = WS_O + 128 * 14 * 1024;
constexpr size_t WS_XM = WS_X1 + 128 * 14 * 1024;
constexpr size_t WS_ST = WS_XM + 128 * 14 * 1024;
constexpr size_t WS_HH = WS_ST + 2048 * 7 * 8;

template <int N> struct IC { static constexpr int v = N; };
template <int I, int N, class F> DI void sfor(F&& f) {
  if constexpr (I < N) { f(IC<I>{}); sfor<I + 1, N>(f); }
}
template <int N> DI void wait_vm_bar() { asm volatile("s_waitcnt vmcnt(%0)\n\ts_barrier" ::"i"(N) : "memory"); }
DI void bar() { asm volatile("s_barrier" ::: "memory"); }
DI void sched_fence() { __builtin_amdgcn_sched_barrier(0); }

DI float bf_lo(unsigned u) { return __uint_as_float(u << 16); }
DI float bf_hi(unsigned u) { return __uint_as_float(u & 0xffff0000u); }
DI unsigned pk_bf16(float lo, float hi) {
  return __builtin_bit_cast(unsigned, __builtin_convertvector(f32x2{lo, hi}, bf16x2));
}
DI unsigned pk_f16(float lo, float hi) {
  return __builtin_bit_cast(unsigned, __builtin_convertvector(f32x2{lo, hi}, f16x2));
}
DI float h_lo(unsigned u) { return (float)__builtin_bit_cast(f16x2, u)[0]; }
DI float h_hi(unsigned u) { return (float)__builtin_bit_cast(f16x2, u)[1]; }
DI float bf_round(float x) { return bf_lo(pk_bf16(x, 0.f)); }
DI float bf2f(u16 v) { return __uint_as_float(((unsigned)v) << 16); }

DI void glds(const void* sbase, unsigned voff, unsigned laddr) {
  asm volatile("s_mov_b32 m0, %2\n\ts_nop 0\n\tglobal_load_lds_dwordx4 %0, %1" ::"v"(voff), "s"(sbase), "s"(laddr));
}
DI unsigned lds_addr(const lds_char* p) { return (unsigned)(uintptr_t)p; }
DI u32x4 lds_rd128(const lds_char* p) { return *(const __attribute__((address_space(3))) u32x4*)p; }
DI f32x4 lds_rd128f(const lds_char* p) { return *(const __attribute__((address_space(3))) f32x4*)p; }
DI u32x2 lds_tr(const lds_char* p) {
  return __builtin_bit_cast(u32x2, __builtin_amdgcn_ds_read_tr16_b64_v4i16((__attribute__((address_space(3))) s16x4*)p));
}
DI f32x4 mfma16(u32x4 a, u32x4 b, f32x4 c) {
  return __builtin_amdgcn_mfma_f32_16x16x32_bf16(__builtin_bit_cast(bf16x8, a), __builtin_bit_cast(bf16x8, b), c, 0, 0, 0);
}

__global__ __launch_bounds__(256) void k0_mod_ln(const u16* __restrict__ x, const u16* __restrict__ c,
                                                 const u16* __restrict__ wmod, char* __restrict__ ws) {
  __shared__ float red[64 * 33];
  const int tid = threadIdx.x;
  TDECL; TS(0);
  if (blockIdx.x < 84) {
    const int n0 = blockIdx.x * 32;
    const int cq = tid & 3, rg = tid >> 2;
    u32x4 w[7];
    float cv[7];
#pragma unroll
    for (int i = 0; i < 7; ++i) {
      w[i] = *(const u32x4*)(wmod + (size_t)(rg + 64 * i) * 2688 + n0 + cq * 8);
      cv[i] = bf2f(c[rg + 64 * i]);
    }
    float acc[8];
#pragma unroll
    for (int j = 0; j < 8; ++j) acc[j] = 0.f;
#pragma unroll
    for (int i = 0; i < 7; ++i) {
      const float s = bf_round(cv[i] / (1.f + __expf(-cv[i])));
#pragma unroll
      for (int j = 0; j < 4; ++j) {
        acc[2 * j] += s * bf_lo(w[i][j]);
        acc[2 * j + 1] += s * bf_hi(w[i][j]);
      }
    }
#pragma unroll
    for (int j = 0; j < 8; ++j) red[rg * 33 + cq * 8 + j] = acc[j];
    TS(1);
    __syncthreads();
    const int col = tid >> 3, part = tid & 7;
    float s = 0.f;
#pragma unroll
    for (int i = 0; i < 8; ++i) s += red[(part * 8 + i) * 33 + col];
    s += __shfl_xor(s, 1);
    s += __shfl_xor(s, 2);
    s += __shfl_xor(s, 4);
    if (part == 0) {
      const int n = n0 + col;
      float v = bf_round(s);
      const int sec = n / 448;
      if (sec == 1 || sec == 4) v = bf_round(1.f + v);
      ((float*)(ws + WS_MOD))[n] = v;
    }
  } else {
    const int lane = tid & 63, wid = tid >> 6;
    const int row = (blockIdx.x - 84) * 32 + wid * 8 + (lane >> 3);
    const int i8 = lane & 7;
    u32x4 v[7];
#pragma unroll
    for (int m = 0; m < 7; ++m) v[m] = *(const u32x4*)(x + (size_t)row * 448 + (i8 + 8 * m) * 8);
    float s = 0.f;
#pragma unroll
    for (int m = 0; m < 7; ++m)
#pragma unroll
      for (int j = 0; j < 4; ++j) s += bf_lo(v[m][j]) + bf_hi(v[m][j]);
    s += __shfl_xor(s, 1);
    s += __shfl_xor(s, 2);
    s += __shfl_xor(s, 4);
    const float mean = s * (1.f / 448.f);
    TS(1);
    float q = 0.f;
#pragma unroll
    for (int m = 0; m < 7; ++m)
#pragma unroll
      for (int j = 0; j < 4; ++j) {
        const float a = bf_lo(v[m][j]) - mean, b = bf_hi(v[m][j]) - mean;
        q += a * a + b * b;
      }
    q += __shfl_xor(q, 1);
    q += __shfl_xor(q, 2);
    q += __shfl_xor(q, 4);
    const float rstd = rsqrtf(q * (1.f / 448.f) + 1e-5f);
    char* dst = ws + WS_XN + (size_t)(row >> 4) * 14 * 1024 + (row & 15) * 16;
#pragma unroll
    for (int m = 0; m < 7; ++m) {
      u32x4 o;
#pragma unroll
      for (int j = 0; j < 4; ++j) o[j] = pk_f16((bf_lo(v[m][j]) - mean) * rstd, (bf_hi(v[m][j]) - mean) * rstd);
      const int ch = i8 + 8 * m;
      *(u32x4*)(dst + (ch >> 2) * 1024 + (ch & 3) * 256) = o;
    }
  }
  TS(41); TFLUSH(0);
}

template <int OFF> DI void glds_o(const void* sbase, unsigned voff, unsigned laddr) {
  asm volatile("s_add_u32 m0, %2, %3\n\ts_nop 0\n\tglobal_load_lds_dwordx4 %0, %1" ::"v"(voff), "s"(sbase), "s"(laddr), "i"(OFF) : "scc");
}
template <int OFF> DI void glds_o48(const void* sbase, unsigned voff, unsigned laddr) {
  asm volatile("s_add_u32 m0, %2, %3\n\ts_mov_b32 exec_hi, 0xffff\n\tglobal_load_lds_dwordx4 %0, %1\n\ts_mov_b32 exec_hi, -1"
               ::"v"(voff), "s"(sbase), "s"(laddr), "i"(OFF) : "scc");
}

template <int RB, int NSEG, int NBS, int KS, int SK, int D, int LDW, bool PERM, bool SWAP>
struct Gemm {
  static constexpr int NST = KS / SK;
  static constexpr int NSLOT = D + 3;
  static constexpr int RS = 32 * NBS;
  static constexpr int PPK = 4 * RB + NSEG * NBS;
  static constexpr int STG = SK * PPK * 1024;
  static constexpr int LDS_BYTES = NSLOT * STG;
  static constexpr int NG = NSEG * NBS;
  static constexpr int NBI = NBS == 7 ? 2 : 1;
  static constexpr int CNT = SK * (RB + NSEG * NBI);
  static_assert(D + 1 < NST && D * CNT < 64 && (SK == 1 || SK == 2) && KS % 2 == 0, "cfg");

  static DI void load(int wl, unsigned lds0, const char* Ablk, const u16* W0, const u16* W1, int n0, int lane TARG) {
    unsigned va[SK][RB], vb[SK][NBI];
#pragma unroll
    for (int i = 0; i < NBI; ++i) {
      const int e = wl * (NBS * 16) + i * 64 + lane;
      int pos, col;
      if constexpr (NBS == 7) {
        pos = (e * 4682) >> 16;
        col = (e - pos * 14) * 8;
        pos &= 31;
      } else {
        pos = e >> 3;
        const int slot = e & 7;
        col = 16 * ((slot >> 1) ^ ((pos >> 1) & 3)) + 8 * (slot & 1);
      }
      const int kk = PERM ? (8 * ((pos >> 2) & 3) + 4 * (pos >> 4) + (pos & 3)) : pos;
#pragma unroll
      for (int k = 0; k < SK; ++k) vb[k][i] = ((k * 32 + kk) * LDW + n0 + col) * 2;
    }
#pragma unroll
    for (int k = 0; k < SK; ++k)
#pragma unroll
      for (int r = 0; r < RB; ++r) va[k][r] = ((wl * RB + r) * KS + k) * 1024 + lane * 16;
    const unsigned mA = lds0 + wl * RB * 1024, mB = lds0 + 4 * RB * 1024 + wl * NBS * 256;
    const char* pa = Ablk;
    const char* p0 = (const char*)W0;
    const char* p1 = (const char*)W1;
    unsigned so = 0;
    auto issue = [&]() {
      const unsigned a = mA + so, b = mB + so;
      sfor<0, SK>([&](auto kk) {
        constexpr int k = decltype(kk)::v;
        sfor<0, RB>([&](auto rr) { glds_o<(k * PPK + decltype(rr)::v) * 1024>(pa, va[k][decltype(rr)::v], a); });
        glds_o<k * PPK * 1024>(p0, vb[k][0], b);
        if constexpr (NBI == 2) glds_o48<k * PPK * 1024 + 1024>(p0, vb[k][1], b);
        if constexpr (NSEG == 2) {
          glds_o<(k * PPK + NBS) * 1024>(p1, vb[k][0], b);
          if constexpr (NBI == 2) glds_o48<(k * PPK + NBS) * 1024 + 1024>(p1, vb[k][1], b);
        }
      });
      pa += SK * 1024;
      p0 += SK * 32 * LDW * 2;
      p1 += SK * 32 * LDW * 2;
      so += STG;
      so = so == NSLOT * STG ? 0 : so;
    };
#pragma unroll 1
    for (int T = -(D + 1); T < NST - D - 1; ++T) {
      if (T >= 0) wait_vm_bar<D * CNT>();
      issue();
    }
    TS(1);
    sfor<0, D + 1>([&](auto tt) { wait_vm_bar<(D - decltype(tt)::v) * CNT>(); });
    bar();
  }

  template <class XR, class XF>
  static DI void compute(const lds_char* lds, f32x4 (&acc)[RB][NG], int wid, int lane, XR&& xr, XF&& xf TARG) {
    constexpr int NBP = NBS == 7 ? 1 : 4;
    const unsigned ap0 = lds_addr(lds) + wid * RB * 1024 + lane * 16;
    unsigned bp0[NBP];
    if constexpr (NBS == 7) {
      bp0[0] = lds_addr(lds) + 4 * RB * 1024 + (lane >> 2) * 224 + 8 * (lane & 3);
    } else {
      const unsigned tb = (lane >> 2) * 128 + (((lane >> 3) & 3) << 5) + 8 * (lane & 3);
#pragma unroll
      for (int g = 0; g < 4; ++g) bp0[g] = lds_addr(lds) + 4 * RB * 1024 + (tb ^ (g << 5));
    }
    unsigned ap = ap0, bp[NBP];
#pragma unroll
    for (int g = 0; g < NBP; ++g) bp[g] = bp0[g];
    u32x4 a[2][RB];
    u32x4 b[SK][NG];
    auto rdA = [&](auto hh, auto kk) {
#pragma unroll
      for (int rb = 0; rb < RB; ++rb)
        a[decltype(hh)::v][rb] = lds_rd128((const lds_char*)(uintptr_t)ap + (decltype(kk)::v * PPK + rb) * 1024);
    };
    auto rdB = [&](auto kk, auto gg) {
      constexpr int k = decltype(kk)::v, g = decltype(gg)::v;
      constexpr int off = (k * PPK + (g / NBS) * NBS) * 1024;
      const lds_char* p;
      if constexpr (NBS == 7) p = (const lds_char*)(uintptr_t)bp[0] + off + (g % NBS) * 32;
      else p = (const lds_char*)(uintptr_t)bp[g % NBS] + off;
      const u32x2 b0 = lds_tr(p), b1 = lds_tr(p + 16 * RS);
      b[k][g] = u32x4{b0[0], b0[1], b1[0], b1[1]};
    };
#pragma unroll
    for (int rb = 0; rb < RB; ++rb)
#pragma unroll
      for (int g = 0; g < NG; ++g) asm volatile("" : "+v"(acc[rb][g]));
    TS(1);
    bar();
    TS(2); TC(50);
    sfor<0, SK>([&](auto kk) {
      rdA(kk, kk);
      if constexpr (SK == 1) xr(a[0]);
      sfor<0, NG>([&](auto gg) { rdB(kk, gg); });
    });
    __builtin_amdgcn_s_waitcnt(0xc07f);
    sfor<0, NG>([&](auto gg) { xf(gg, a[0]); });
    unsigned so = 0;
#pragma unroll 1
    for (int it = 0; it < KS / 2; ++it) {
      sfor<0, 2>([&](auto hh) {
        constexpr int h = decltype(hh)::v;
        constexpr int kb = SK == 1 ? 0 : h;
        if constexpr (SK == 1 || h == 0) {
          so += STG;
          so = so == NSLOT * STG ? 0 : so;
          ap = ap0 + so;
#pragma unroll
          for (int g = 0; g < NBP; ++g) bp[g] = bp0[g] + so;
          bar();
        }
        if constexpr (SK == 1) {
          rdA(IC<h ^ 1>{}, IC<0>{});
          xr(a[h ^ 1]);
        }
        sched_fence();
        sfor<0, NG>([&](auto gg) {
          constexpr int g = decltype(gg)::v;
#pragma unroll
          for (int rb = 0; rb < RB; ++rb)
            acc[rb][g] = SWAP ? mfma16(b[kb][g], a[h][rb], acc[rb][g]) : mfma16(a[h][rb], b[kb][g], acc[rb][g]);
          rdB(IC<kb>{}, gg);
          if constexpr (SK == 2 && g == NG - 1) rdA(hh, hh);
          xf(gg, a[h ^ 1]);
          sched_fence();
        });
      });
    }
  }
};

struct NoX {
  template <class A> DI void operator()(A&) const {}
  template <class A, class B> DI void operator()(A, B&) const {}
};

constexpr int D1 = 4;
typedef Gemm<2, 1, 7, 14, 1, D1, 1344, true, true> G1S;

template <bool SWAP>
DI void k1_compute(const lds_char* lds, char* __restrict__ ws, int mt, int nt, int wid, int lane TARG) {
  typedef Gemm<2, 1, 7, 14, 1, D1, 1344, true, SWAP> G;
  const lds_char* vec = lds + G::LDS_BYTES;
  f32x4 acc[2][7];
#pragma unroll
  for (int rb = 0; rb < 2; ++rb)
#pragma unroll
    for (int nb = 0; nb < 7; ++nb) acc[rb][nb] = f32x4{0.f, 0.f, 0.f, 0.f};
  const int gq = lane >> 4, t = lane & 15;
  const lds_char* vp = vec + gq * 32;
  f32x4 sv[2], mv[2];
  G::compute(
      lds, acc, wid, lane,
      [&](u32x4(&)[2]) {
        sv[0] = lds_rd128f(vp);
        sv[1] = lds_rd128f(vp + 16);
        mv[0] = lds_rd128f(vp + 1792);
        mv[1] = lds_rd128f(vp + 1792 + 16);
        vp += 128;
      },
      [&](auto gg, u32x4(&a)[2]) {
        constexpr int g = decltype(gg)::v;
        if constexpr (g >= 3) {
          constexpr int j = g - 3;
#pragma unroll
          for (int rb = 0; rb < 2; ++rb)
            a[rb][j] = pk_bf16(h_lo(a[rb][j]) * mv[j >> 1][(j & 1) * 2] + sv[j >> 1][(j & 1) * 2],
                               h_hi(a[rb][j]) * mv[j >> 1][(j & 1) * 2 + 1] + sv[j >> 1][(j & 1) * 2 + 1]);
        }
      } TPASS);
  TS(40); TC(51);
  const int sec = nt >> 2, h0 = (nt & 3) * 4;
  if constexpr (SWAP) {
    const float sc = sec == 0 ? 0.18898223650461362f * 1.4426950408889634f : 1.f;
    char* base = ws + (sec == 0 ? WS_Q : WS_K);
#pragma unroll
    for (int rb = 0; rb < 2; ++rb) {
      const int tb = mt * 8 + wid * 2 + rb;
#pragma unroll
      for (int nb = 0; nb < 7; ++nb) {
        const int Gi = 4 * nb + gq;
        const int hd = (Gi * 37) >> 8, dg = Gi - 7 * hd;
        char* dst = base + ((size_t)(h0 + hd) * 128 + tb) * 1024 + ((dg >> 1) * 16 + t) * 16 + (dg & 1) * 8;
        const f32x4 v = acc[rb][nb];
        const unsigned lo = pk_bf16(v[0] * sc, v[1] * sc), hi = pk_bf16(v[2] * sc, v[3] * sc);
        if (dg == 6) *(u32x4*)dst = u32x4{lo, hi, 0u, 0u};
        else *(u32x2*)dst = u32x2{lo, hi};
      }
    }
  } else {
    const int s32 = mt * 4 + wid;
    char* base = ws + WS_V;
#pragma unroll
    for (int nb = 0; nb < 7; ++nb) {
      const int n = 16 * nb + t;
      const int hd = (n * 147) >> 12, d = n - 28 * hd;
      char* dst = base + (((size_t)(h0 + hd) * 64 + s32) * 2 + (d >> 4)) * 1024 + (gq * 16 + (d & 15)) * 16;
      const f32x4 v0 = acc[0][nb], v1 = acc[1][nb];
      *(u32x4*)dst = u32x4{pk_bf16(v0[0], v0[1]), pk_bf16(v0[2], v0[3]), pk_bf16(v1[0], v1[1]), pk_bf16(v1[2], v1[3])};
    }
    if (lane < 16) {
      char* dst = base + (((size_t)(h0 + (lane >> 2)) * 64 + s32) * 2 + 1) * 1024 + ((lane & 3) * 16 + 12) * 16;
      *(u32x4*)dst = u32x4{0x3f803f80u, 0x3f803f80u, 0x3f803f80u, 0x3f803f80u};
    }
  }
}

__global__ __launch_bounds__(512, 1) void k1_qkv(const u16* __restrict__ wqkv, const u16* __restrict__ wg,
                                                 const u16* __restrict__ wu, char* __restrict__ ws) {
  TDECL; TS(0);
  extern __shared__ __attribute__((aligned(16))) char smem[];
  lds_char* lds = (lds_char*)smem;
  const int lane = threadIdx.x & 63, wid = __builtin_amdgcn_readfirstlane(threadIdx.x >> 6);
  const int bid = blockIdx.x, xcd = bid & 7, j = bid >> 3;
  if (j >= 24) {
    const int e = (j - 24) * 8 + xcd;
    const int mat = e >> 5, c0 = (e & 31) * 56;
    const u16* W = mat ? wu : wg;
    const int tid = threadIdx.x;
    float* red = (float*)smem;
    if (tid < 448) {
      const int rg = tid / 7, cq = tid - rg * 7;
      u32x4 w[7];
      float m[7], sb[7];
#pragma unroll
      for (int i = 0; i < 7; ++i) {
        const int k = rg + 64 * i;
        w[i] = *(const u32x4*)(W + (size_t)k * 1792 + c0 + cq * 8);
        sb[i] = ((const float*)(ws + WS_MOD))[3 * 448 + k];
        m[i] = ((const float*)(ws + WS_MOD))[4 * 448 + k];
      }
      float a1[8], a2[8];
#pragma unroll
      for (int q = 0; q < 8; ++q) a1[q] = a2[q] = 0.f;
#pragma unroll
      for (int i = 0; i < 7; ++i)
#pragma unroll
        for (int q = 0; q < 4; ++q) {
          a1[2 * q] += m[i] * bf_lo(w[i][q]);
          a1[2 * q + 1] += m[i] * bf_hi(w[i][q]);
          a2[2 * q] += sb[i] * bf_lo(w[i][q]);
          a2[2 * q + 1] += sb[i] * bf_hi(w[i][q]);
        }
#pragma unroll
      for (int q = 0; q < 8; ++q) {
        red[rg * 113 + cq * 8 + q] = a1[q];
        red[rg * 113 + 56 + cq * 8 + q] = a2[q];
      }
    }
    __syncthreads();
    if (tid < 448) {
      const int col = tid >> 2, part = tid & 3;
      float s = 0.f;
#pragma unroll
      for (int i = 0; i < 16; ++i) s += red[(part * 16 + i) * 113 + col];
      s += __shfl_xor(s, 1);
      s += __shfl_xor(s, 2);
      if (part == 0) {
        const int which = col >= 56, n = c0 + col - which * 56;
        const int nt = n / 112;
        ((float*)(ws + WS_U))[(nt * 4 + mat * 2 + which) * 112 + n - nt * 112] = s;
      }
    }
    TS(41); TFLUSH(1);
    return;
  }
  const int mt = (xcd >> 1) * 4 + (j & 3), nt = (xcd & 1) * 6 + (j >> 2);
  if (wid >= 4) {
    const int off = (wid - 4) * 1024 + lane * 16;
    glds(ws + WS_MOD, off < 3584 ? off : 0, lds_addr(lds) + G1S::LDS_BYTES + (wid - 4) * 1024);
    G1S::load(wid - 4, lds_addr(lds), ws + WS_XN + (size_t)mt * 8 * 14 * 1024, wqkv, wqkv, nt * 112, lane TPASS);
  } else {
    if (nt < 8) k1_compute<true>(lds, ws, mt, nt, wid, lane TPASS);
    else k1_compute<false>(lds, ws, mt, nt, wid, lane TPASS);
  }
  TS(41); TFLUSH(1);
}

DI void k2_wave(const lds_char* lds, char* __restrict__ ws, int qt, int head, int wid, int lane) {
  const int gq = lane >> 4, t = lane & 15;
  const int lp0 = wid >> 1;
  const int qb = 8 * qt + wid;
  const lds_char* kp = lds + lp0 * 2048 + lane * 16;
  const lds_char* vp = lds + 24576 + lp0 * 2048 + lane * 16;
  const u32x4 qf = lds_rd128(lds + 49152 + wid * 1024 + lane * 16);
  f32x4 sc[18];
  const float NEG = -__builtin_inff();
#pragma unroll
  for (int i = 0; i < 18; ++i) {
    const u32x4 kf = lds_rd128(kp + i * 1024);
    sc[i] = mfma16(kf, qf, f32x4{0.f, 0.f, 0.f, 0.f});
  }
  const int d0 = 256 + 16 * (wid & 1) + t - 4 * gq;
#pragma unroll
  for (int r = 0; r < 4; ++r) {
    if (d0 - r >= 256) sc[0][r] = NEG;
    if (d0 - 16 - r >= 256) sc[1][r] = NEG;
    if (d0 - 256 - r < 0) sc[16][r] = NEG;
    if (d0 - 272 - r < 0) sc[17][r] = NEG;
  }
  if (qt < 2) {
    const int kb0 = 8 * qt - 16 + 2 * lp0;
#pragma unroll
    for (int i = 0; i < 16; ++i)
      if (kb0 + i < 0) sc[i] = f32x4{NEG, NEG, NEG, NEG};
  }
  float m = sc[16][0];
#pragma unroll
  for (int i = 0; i < 18; ++i) m = fmaxf(fmaxf(m, fmaxf(sc[i][0], sc[i][1])), fmaxf(sc[i][2], sc[i][3]));
  m = fmaxf(m, __shfl_xor(m, 16));
  m = fmaxf(m, __shfl_xor(m, 32));
  unsigned pp[18][2];
#pragma unroll
  for (int i = 0; i < 18; ++i) {
    pp[i][0] = pk_bf16(__builtin_amdgcn_exp2f(sc[i][0] - m), __builtin_amdgcn_exp2f(sc[i][1] - m));
    pp[i][1] = pk_bf16(__builtin_amdgcn_exp2f(sc[i][2] - m), __builtin_amdgcn_exp2f(sc[i][3] - m));
  }
  wait_vm_bar<0>();
  f32x4 o0 = {0.f, 0.f, 0.f, 0.f}, o1 = {0.f, 0.f, 0.f, 0.f};
#pragma unroll
  for (int p = 0; p < 9; ++p) {
    const u32x4 pf = {pp[2 * p][0], pp[2 * p][1], pp[2 * p + 1][0], pp[2 * p + 1][1]};
    const u32x4 v0 = lds_rd128(vp + p * 2048), v1 = lds_rd128(vp + p * 2048 + 1024);
    o0 = mfma16(v0, pf, o0);
    o1 = mfma16(v1, pf, o1);
  }
  const float sum = __shfl(o1[0], 48 + t);
  const float inv = __builtin_amdgcn_rcpf(sum);
  char* base = ws + WS_O + (size_t)qb * 14 * 1024;
  {
    const int Gi = 7 * head + gq;
    *(u32x2*)(base + (Gi >> 3) * 1024 + (((Gi >> 1) & 3) * 16 + t) * 16 + (Gi & 1) * 8) =
        u32x2{pk_bf16(o0[0] * inv, o0[1] * inv), pk_bf16(o0[2] * inv, o0[3] * inv)};
  }
  if (gq < 3) {
    const int Gi = 7 * head + 4 + gq;
    *(u32x2*)(base + (Gi >> 3) * 1024 + (((Gi >> 1) & 3) * 16 + t) * 16 + (Gi & 1) * 8) =
        u32x2{pk_bf16(o1[0] * inv, o1[1] * inv), pk_bf16(o1[2] * inv, o1[3] * inv)};
  }
}

__global__ __launch_bounds__(512, 2) void k2_attn(char* __restrict__ ws) {
  TDECL; TS(0);
  extern __shared__ __attribute__((aligned(16))) char smem[];
  lds_char* lds = (lds_char*)smem;
  const int lane = threadIdx.x & 63, wid = __builtin_amdgcn_readfirstlane(threadIdx.x >> 6);
  const int bid = blockIdx.x, xcd = bid & 7, j = bid >> 3;
  const int head = xcd * 2 + (j & 1), qt = j >> 1;
  const unsigned l0 = lds_addr(lds);
  glds(ws + WS_Q + ((size_t)head * 128 + 8 * qt + wid) * 1024, lane * 16, l0 + 49152 + wid * 1024);
#pragma unroll
  for (int i = 0; i < 3; ++i) {
    const int lk = wid * 3 + i;
    int tb = 8 * qt - 16 + lk;
    tb = tb < 0 ? 0 : tb;
    glds(ws + WS_K + ((size_t)head * 128 + tb) * 1024, lane * 16, l0 + lk * 1024);
  }
#pragma unroll
  for (int i = 0; i < 3; ++i) {
    const int lv = wid * 3 + i;
    int f = (4 * qt - 8) * 2 + lv;
    f = f < 0 ? (lv & 1) : f;
    glds(ws + WS_V + ((size_t)head * 128 + f) * 1024, lane * 16, l0 + 24576 + lv * 1024);
  }
  TS(1);
  wait_vm_bar<3>();
  TS(2);
  k2_wave(lds, ws, qt, head, wid, lane);
  TS(41); TFLUSH(2);
}

typedef Gemm<1, 1, 4, 14, 2, 4, 448, true, true> G3;
__global__ __launch_bounds__(512, 1) void k3_wo(const u16* __restrict__ x, const u16* __restrict__ wo, char* __restrict__ ws) {
  TDECL; TS(0);
  extern __shared__ __attribute__((aligned(16))) char smem[];
  lds_char* lds = (lds_char*)smem;
  const int lane = threadIdx.x & 63, wid = __builtin_amdgcn_readfirstlane(threadIdx.x >> 6);
  const int bid = blockIdx.x, xcd = bid & 7, j = bid >> 3;
  const int mt = xcd * 4 + (j & 3), nt = j >> 2;
  if (wid >= 4) {
    G3::load(wid - 4, lds_addr(lds), ws + WS_O + (size_t)mt * 4 * 14 * 1024, wo, wo, nt * 64, lane TPASS);
    TS(41); TFLUSH(3);
    return;
  }
  const int gq = lane >> 4, t = lane & 15;
  const int tb = mt * 4 + wid, row = tb * 16 + t;
  u32x2 xr[4];
  f32x4 ga[4], m2[4];
#pragma unroll
  for (int nb = 0; nb < 4; ++nb) {
    xr[nb] = *(const u32x2*)(x + (size_t)row * 448 + nt * 64 + nb * 16 + gq * 4);
    ga[nb] = *(const f32x4*)((const float*)(ws + WS_MOD) + 2 * 448 + nt * 64 + nb * 16 + gq * 4);
    m2[nb] = *(const f32x4*)((const float*)(ws + WS_MOD) + 4 * 448 + nt * 64 + nb * 16 + gq * 4);
  }
  f32x4 acc[1][4];
#pragma unroll
  for (int nb = 0; nb < 4; ++nb) acc[0][nb] = f32x4{0.f, 0.f, 0.f, 0.f};
  G3::compute(lds, acc, wid, lane, NoX{}, NoX{} TPASS);
  TS(40); TC(51);
  unsigned o[4][2], om[4][2];
  float v[4][4];
  float s = 0.f;
#pragma unroll
  for (int nb = 0; nb < 4; ++nb) {
    o[nb][0] = pk_bf16(bf_lo(xr[nb][0]) + ga[nb][0] * acc[0][nb][0], bf_hi(xr[nb][0]) + ga[nb][1] * acc[0][nb][1]);
    o[nb][1] = pk_bf16(bf_lo(xr[nb][1]) + ga[nb][2] * acc[0][nb][2], bf_hi(xr[nb][1]) + ga[nb][3] * acc[0][nb][3]);
    v[nb][0] = bf_lo(o[nb][0]); v[nb][1] = bf_hi(o[nb][0]); v[nb][2] = bf_lo(o[nb][1]); v[nb][3] = bf_hi(o[nb][1]);
    om[nb][0] = pk_bf16(v[nb][0] * m2[nb][0], v[nb][1] * m2[nb][1]);
    om[nb][1] = pk_bf16(v[nb][2] * m2[nb][2], v[nb][3] * m2[nb][3]);
    s += (v[nb][0] + v[nb][1]) + (v[nb][2] + v[nb][3]);
  }
  char* dst = ws + WS_X1 + ((size_t)tb * 14 + nt * 2) * 1024 + lane * 16;
  *(u32x4*)(dst + (WS_XM - WS_X1)) = u32x4{om[0][0], om[0][1], om[1][0], om[1][1]};
  *(u32x4*)(dst + (WS_XM - WS_X1) + 1024) = u32x4{om[2][0], om[2][1], om[3][0], om[3][1]};
  *(u32x4*)dst = u32x4{o[0][0], o[0][1], o[1][0], o[1][1]};
  *(u32x4*)(dst + 1024) = u32x4{o[2][0], o[2][1], o[3][0], o[3][1]};
  s += __shfl_xor(s, 16);
  s += __shfl_xor(s, 32);
  const float mc = s * (1.f / 64.f);
  float q = 0.f;
#pragma unroll
  for (int nb = 0; nb < 4; ++nb)
#pragma unroll
    for (int r = 0; r < 4; ++r) q += (v[nb][r] - mc) * (v[nb][r] - mc);
  q += __shfl_xor(q, 16);
  q += __shfl_xor(q, 32);
  if (gq == 0) *(f32x2*)(ws + WS_ST + ((size_t)row * 7 + nt) * 8) = f32x2{mc, q};
  TS(41); TFLUSH(3);
}

typedef Gemm<2, 2, 7, 14, 1, 3, 1792, false, true> G4;
__global__ __launch_bounds__(512, 1) void k4_gateup(const u16* __restrict__ wg, const u16* __restrict__ wu, char* __restrict__ ws) {
  TDECL; TS(0);
  extern __shared__ __attribute__((aligned(16))) char smem[];
  lds_char* lds = (lds_char*)smem;
  const int lane = threadIdx.x & 63, wid = __builtin_amdgcn_readfirstlane(threadIdx.x >> 6);
  const int bid = blockIdx.x, xcd = bid & 7, j = bid >> 3;
  const int mt = (xcd >> 2) * 8 + (j & 7), nt = (xcd & 3) * 4 + (j >> 3);
  if (wid >= 4) {
    const int wl = wid - 4;
    const unsigned ex = lds_addr(lds) + G4::LDS_BYTES;
    if (wl < 2) {
      const int off = wl * 1024 + lane * 16;
      glds(ws + WS_U + (size_t)nt * 1792, off < 1792 ? off : 0, ex + wl * 1024);
    }
#pragma unroll
    for (int i = 0; i < 2; ++i) {
      const int o2 = (wl * 2 + i) * 1024 + lane * 16;
      glds(ws + WS_ST + (size_t)mt * 7168, o2 < 7168 ? o2 : 0, ex + 2048 + (wl * 2 + i) * 1024);
    }
    G4::load(wl, lds_addr(lds), ws + WS_XM + (size_t)mt * 8 * 14 * 1024, wg, wu, nt * 112, lane TPASS);
    TS(41); TFLUSH(4);
    return;
  }
  const int gq = lane >> 4, t = lane & 15;
  f32x4 acc[2][14];
#pragma unroll
  for (int rb = 0; rb < 2; ++rb)
#pragma unroll
    for (int nb = 0; nb < 14; ++nb) acc[rb][nb] = f32x4{0.f, 0.f, 0.f, 0.f};
  G4::compute(lds, acc, wid, lane, NoX{}, NoX{} TPASS);
  TS(40); TC(51);
  const lds_char* ex = lds + G4::LDS_BYTES;
  const lds_char* up = ex + gq * 16;
  const int B0 = nt * 7;
#pragma unroll 1
  for (int rb = 0; rb < 2; ++rb) {
    const lds_char* sp = ex + 2048 + ((wid * 2 + rb) * 16 + t) * 56;
    float mc[7], q = 0.f, sm = 0.f;
#pragma unroll
    for (int c = 0; c < 7; ++c) {
      const f32x2 e = *(const __attribute__((address_space(3))) f32x2*)(sp + c * 8);
      mc[c] = e[0]; q += e[1]; sm += e[0];
    }
    const float mean = sm * (1.f / 7.f);
#pragma unroll
    for (int c = 0; c < 7; ++c) q += 64.f * (mc[c] - mean) * (mc[c] - mean);
    const float rs = rsqrtf(q * (1.f / 448.f) + 1e-5f);
    const int tb = mt * 8 + wid * 2 + rb;
    char* base = ws + WS_HH + (size_t)tb * 56 * 1024 + lane * 16 + (B0 >> 1) * 1024;
    const f32x2 nm = {-mean, -mean}, rs2 = {rs, rs};
#pragma unroll
    for (int nb = 0; nb < 7; ++nb) {
      const f32x4 u1g = lds_rd128f(up + nb * 64), u2g = lds_rd128f(up + nb * 64 + 448);
      const f32x4 u1u = lds_rd128f(up + nb * 64 + 896), u2u = lds_rd128f(up + nb * 64 + 1344);
      unsigned o[2];
#pragma unroll
      for (int r = 0; r < 2; ++r) {
        const f32x2 ag = {acc[0][nb][2 * r], acc[0][nb][2 * r + 1]}, au = {acc[0][7 + nb][2 * r], acc[0][7 + nb][2 * r + 1]};
        const f32x2 gv = rs2 * (nm * f32x2{u1g[2 * r], u1g[2 * r + 1]} + ag) + f32x2{u2g[2 * r], u2g[2 * r + 1]};
        const f32x2 uv = rs2 * (nm * f32x2{u1u[2 * r], u1u[2 * r + 1]} + au) + f32x2{u2u[2 * r], u2u[2 * r + 1]};
        const f32x2 z = gv * (gv * gv * f32x2{-0.1029432397f, -0.1029432397f} + f32x2{-2.302208198f, -2.302208198f});
        const f32x2 d = f32x2{__builtin_amdgcn_exp2f(z[0]), __builtin_amdgcn_exp2f(z[1])} + f32x2{1.f, 1.f};
        const f32x2 h = gv * uv * f32x2{__builtin_amdgcn_rcpf(d[0]), __builtin_amdgcn_rcpf(d[1])};
        o[r] = pk_bf16(h[0], h[1]);
      }
      const int bo = (B0 & 1) + nb;
      *(u32x2*)(base + (bo >> 1) * 1024 + (bo & 1) * 8) = u32x2{o[0], o[1]};
    }
#pragma unroll
    for (int nb = 0; nb < 14; ++nb) acc[0][nb] = acc[1][nb];
  }
  TS(41); TFLUSH(4);
}

typedef Gemm<1, 1, 4, 56, 2, 4, 448, false, true> G5;
__global__ __launch_bounds__(512, 1) void k5_down(const u16* __restrict__ wd, char* __restrict__ ws, u16* __restrict__ out) {
  TDECL; TS(0);
  extern __shared__ __attribute__((aligned(16))) char smem[];
  lds_char* lds = (lds_char*)smem;
  const int lane = threadIdx.x & 63, wid = __builtin_amdgcn_readfirstlane(threadIdx.x >> 6);
  const int bid = blockIdx.x, xcd = bid & 7, j = bid >> 3;
  const int mt = xcd * 4 + (j & 3), nt = j >> 2;
  if (wid >= 4) {
    G5::load(wid - 4, lds_addr(lds), ws + WS_HH + (size_t)mt * 4 * 56 * 1024, wd, wd, nt * 64, lane TPASS);
    TS(41); TFLUSH(5);
    return;
  }
  const int gq = lane >> 4, t = lane & 15;
  const int tb = mt * 4 + wid, row = tb * 16 + t;
  u32x4 xr[2];
  f32x4 gb[4];
#pragma unroll
  for (int i = 0; i < 2; ++i) xr[i] = *(const u32x4*)(ws + WS_X1 + ((size_t)tb * 14 + nt * 2 + i) * 1024 + lane * 16);
#pragma unroll
  for (int nb = 0; nb < 4; ++nb) gb[nb] = *(const f32x4*)((const float*)(ws + WS_MOD) + 5 * 448 + nt * 64 + nb * 16 + gq * 4);
  f32x4 acc[1][4];
#pragma unroll
  for (int nb = 0; nb < 4; ++nb) acc[0][nb] = f32x4{0.f, 0.f, 0.f, 0.f};
  G5::compute(lds, acc, wid, lane, NoX{}, NoX{} TPASS);
  TS(40); TC(51);
#pragma unroll
  for (int nb = 0; nb < 4; ++nb) {
    const unsigned x0 = xr[nb >> 1][(nb & 1) * 2], x1 = xr[nb >> 1][(nb & 1) * 2 + 1];
    const unsigned lo = pk_bf16(bf_lo(x0) + gb[nb][0] * acc[0][nb][0], bf_hi(x0) + gb[nb][1] * acc[0][nb][1]);
    const unsigned hi = pk_bf16(bf_lo(x1) + gb[nb][2] * acc[0][nb][2], bf_hi(x1) + gb[nb][3] * acc[0][nb][3]);
    *(u32x2*)(out + (size_t)row * 448 + nt * 64 + nb * 16 + gq * 4) = u32x2{lo, hi};
  }
  TS(41); TFLUSH(5);
}

extern "C" void kernel_launch(void* const* d_in, const int* in_sizes, int n_in, void* d_out, int out_size, void* d_ws,
                              size_t ws_size, hipStream_t stream) {
  const u16* x = (const u16*)d_in[0];
  const u16* wqkv = (const u16*)d_in[1];
  const u16* wo = (const u16*)d_in[2];
  const u16* wg = (const u16*)d_in[4];
  const u16* wu = (const u16*)d_in[5];
  const u16* wd = (const u16*)d_in[6];
  const u16* c = (const u16*)d_in[7];
  const u16* wmod = (const u16*)d_in[8];
  char* ws = (char*)d_ws;
  constexpr int L1 = G1S::LDS_BYTES + 4096;
  constexpr int L2 = 49152 + 8192;
  constexpr int L3 = G3::LDS_BYTES;
  constexpr int L4 = G4::LDS_BYTES + 2048 + 8192;
  constexpr int L5 = G5::LDS_BYTES;
  static_assert(L1 <= 163840 && L3 <= 163840 && L4 <= 163840 && L5 <= 163840, "lds");
  (void)hipFuncSetAttribute((const void*)k1_qkv, hipFuncAttributeMaxDynamicSharedMemorySize, L1);
  (void)hipFuncSetAttribute((const void*)k3_wo, hipFuncAttributeMaxDynamicSharedMemorySize, L3);
  (void)hipFuncSetAttribute((const void*)k4_gateup, hipFuncAttributeMaxDynamicSharedMemorySize, L4);
  (void)hipFuncSetAttribute((const void*)k5_down, hipFuncAttributeMaxDynamicSharedMemorySize, L5);
  k0_mod_ln<<<148, 256, 0, stream>>>(x, c, wmod, ws);
  k1_qkv<<<256, 512, L1, stream>>>(wqkv, wg, wu, ws);
  k2_attn<<<256, 512, L2, stream>>>(ws);
  k3_wo<<<224, 512, L3, stream>>>(x, wo, ws);
  k4_gateup<<<256, 512, L4, stream>>>(wg, wu, ws);
  k5_down<<<224, 512, L5, stream>>>(wd, ws, (u16*)d_out);
}
```

```cpp
#include <hip/hip_runtime.h>
#include <hip/hip_bf16.h>
#include <stdint.h>

typedef __attribute__((ext_vector_type(8))) __bf16 bf16x8;
typedef __attribute__((ext_vector_type(2))) __bf16 bf16x2;
typedef __attribute__((ext_vector_type(2))) _Float16 f16x2;
typedef __attribute__((ext_vector_type(4))) float f32x4;
typedef __attribute__((ext_vector_type(2))) float f32x2;
typedef __attribute__((ext_vector_type(4))) short s16x4;
typedef __attribute__((ext_vector_type(4))) unsigned u32x4;
typedef __attribute__((ext_vector_type(2))) unsigned u32x2;
typedef __attribute__((address_space(3))) char lds_char;
typedef unsigned short u16;

#define DI __device__ __forceinline__
#define TDECL
#define TS(i)
#define TC(i)
#define TFLUSH(k)
#define TARG
#define TPASS

constexpr size_t WS_MOD = 0;
constexpr size_t WS_U = 16384;
constexpr size_t WS_XN = 65536;
constexpr size_t WS_Q = WS_XN + 128 * 14 * 1024;
constexpr size_t WS_K = WS_Q + 16 * 128 * 1024;
constexpr size_t WS_V = WS_K + 16 * 128 * 1024;
constexpr size_t WS_O = WS_V + 16 * 128 * 1024;
constexpr size_t WS_X1 = WS_O + 128 * 14 * 1024;
constexpr size_t WS_XM = WS_X1 + 128 * 14 * 1024;
constexpr size_t WS_ST = WS_XM + 128 * 14 * 1024;
constexpr size_t WS_HH = WS_ST + 2048 * 7 * 8;

template <int N> struct IC { static constexpr int v = N; };
template <int I, int N, class F> DI void sfor(F&& f) {
  if constexpr (I < N) { f(IC<I>{}); sfor<I + 1, N>(f); }
}
template <int N> DI void wait_vm_bar() { asm volatile("s_waitcnt vmcnt(%0)\n\ts_barrier" ::"i"(N) : "memory"); }
DI void bar() { asm volatile("s_barrier" ::: "memory"); }
DI void sched_fence() { __builtin_amdgcn_sched_barrier(0); }

DI float bf_lo(unsigned u) { return __uint_as_float(u << 16); }
DI float bf_hi(unsigned u) { return __uint_as_float(u & 0xffff0000u); }
DI unsigned pk_bf16(float lo, float hi) {
  return __builtin_bit_cast(unsigned, __builtin_convertvector(f32x2{lo, hi}, bf16x2));
}
DI unsigned pk_f16(float lo, float hi) {
  return __builtin_bit_cast(unsigned, __builtin_convertvector(f32x2{lo, hi}, f16x2));
}
DI float h_lo(unsigned u) { return (float)__builtin_bit_cast(f16x2, u)[0]; }
DI float h_hi(unsigned u) { return (float)__builtin_bit_cast(f16x2, u)[1]; }
DI float mix_lo(unsigned h, float m, float a) {
  float r;
  asm("v_fma_mix_f32 %0, %1, %2, %3 op_sel_hi:[1,0,0]" : "=v"(r) : "v"(h), "v"(m), "v"(a));
  return r;
}
DI float mix_hi(unsigned h, float m, float a) {
  float r;
  asm("v_fma_mix_f32 %0, %1, %2, %3 op_sel:[1,0,0] op_sel_hi:[1,0,0]" : "=v"(r) : "v"(h), "v"(m), "v"(a));
  return r;
}
DI float bf_round(float x) { return bf_lo(pk_bf16(x, 0.f)); }
DI float bf2f(u16 v) { return __uint_as_float(((unsigned)v) << 16); }

DI void glds(const void* sbase, unsigned voff, unsigned laddr) {
  asm volatile("s_mov_b32 m0, %2\n\ts_nop 0\n\tglobal_load_lds_dwordx4 %0, %1" ::"v"(voff), "s"(sbase), "s"(laddr));
}
DI void pf(unsigned& d, const void* p) {}
DI void pf_done(unsigned d) { asm volatile("s_waitcnt vmcnt(0)" ::"v"(d)); }
#define STP "sc1"
DI void st128(void* p, u32x4 v) { asm volatile("global_store_dwordx4 %0, %1, off " STP "\n\ts_nop 1" ::"v"(p), "v"(v) : "memory"); }
DI void st64(void* p, u32x2 v) { asm volatile("global_store_dwordx2 %0, %1, off " STP "\n\ts_nop 1" ::"v"(p), "v"(v) : "memory"); }
DI unsigned lds_addr(const lds_char* p) { return (unsigned)(uintptr_t)p; }
DI u32x4 lds_rd128(const lds_char* p) { return *(const __attribute__((address_space(3))) u32x4*)p; }
DI f32x4 lds_rd128f(const lds_char* p) { return *(const __attribute__((address_space(3))) f32x4*)p; }
DI u32x2 lds_tr(const lds_char* p) {
  return __builtin_bit_cast(u32x2, __builtin_amdgcn_ds_read_tr16_b64_v4i16((__attribute__((address_space(3))) s16x4*)p));
}
DI f32x4 mfma16(u32x4 a, u32x4 b, f32x4 c) {
  return __builtin_amdgcn_mfma_f32_16x16x32_bf16(__builtin_bit_cast(bf16x8, a), __builtin_bit_cast(bf16x8, b), c, 0, 0, 0);
}

__global__ __launch_bounds__(256) void k0_mod_ln(const u16* __restrict__ x, const u16* __restrict__ c,
                                                 const u16* __restrict__ wmod, char* __restrict__ ws) {
  __shared__ float red[64 * 33];
  const int tid = threadIdx.x;
  TDECL; TS(0);
  if (blockIdx.x < 84) {
    const int n0 = blockIdx.x * 32;
    const int cq = tid & 3, rg = tid >> 2;
    u32x4 w[7];
    float cv[7];
#pragma unroll
    for (int i = 0; i < 7; ++i) {
      w[i] = *(const u32x4*)(wmod + (size_t)(rg + 64 * i) * 2688 + n0 + cq * 8);
      cv[i] = bf2f(c[rg + 64 * i]);
    }
    float acc[8];
#pragma unroll
    for (int j = 0; j < 8; ++j) acc[j] = 0.f;
#pragma unroll
    for (int i = 0; i < 7; ++i) {
      const float s = bf_round(cv[i] / (1.f + __expf(-cv[i])));
#pragma unroll
      for (int j = 0; j < 4; ++j) {
        acc[2 * j] += s * bf_lo(w[i][j]);
        acc[2 * j + 1] += s * bf_hi(w[i][j]);
      }
    }
#pragma unroll
    for (int j = 0; j < 8; ++j) red[rg * 33 + cq * 8 + j] = acc[j];
    TS(1);
    __syncthreads();
    const int col = tid >> 3, part = tid & 7;
    float s = 0.f;
#pragma unroll
    for (int i = 0; i < 8; ++i) s += red[(part * 8 + i) * 33 + col];
    s += __shfl_xor(s, 1);
    s += __shfl_xor(s, 2);
    s += __shfl_xor(s, 4);
    if (part == 0) {
      const int n = n0 + col;
      float v = bf_round(s);
      const int sec = n / 448;
      if (sec == 1 || sec == 4) v = bf_round(1.f + v);
      ((float*)(ws + WS_MOD))[n] = v;
    }
  } else {
    const int lane = tid & 63, wid = tid >> 6;
    const int row = (blockIdx.x - 84) * 32 + wid * 8 + (lane >> 3);
    const int i8 = lane & 7;
    u32x4 v[7];
#pragma unroll
    for (int m = 0; m < 7; ++m) v[m] = *(const u32x4*)(x + (size_t)row * 448 + (i8 + 8 * m) * 8);
    float s = 0.f;
#pragma unroll
    for (int m = 0; m < 7; ++m)
#pragma unroll
      for (int j = 0; j < 4; ++j) s += bf_lo(v[m][j]) + bf_hi(v[m][j]);
    s += __shfl_xor(s, 1);
    s += __shfl_xor(s, 2);
    s += __shfl_xor(s, 4);
    const float mean = s * (1.f / 448.f);
    TS(1);
    float q = 0.f;
#pragma unroll
    for (int m = 0; m < 7; ++m)
#pragma unroll
      for (int j = 0; j < 4; ++j) {
        const float a = bf_lo(v[m][j]) - mean, b = bf_hi(v[m][j]) - mean;
        q += a * a + b * b;
      }
    q += __shfl_xor(q, 1);
    q += __shfl_xor(q, 2);
    q += __shfl_xor(q, 4);
    const float rstd = rsqrtf(q * (1.f / 448.f) + 1e-5f);
    char* dst = ws + WS_XN + (size_t)(row >> 4) * 14 * 1024 + (row & 15) * 16;
#pragma unroll
    for (int m = 0; m < 7; ++m) {
      u32x4 o;
#pragma unroll
      for (int j = 0; j < 4; ++j) o[j] = pk_f16((bf_lo(v[m][j]) - mean) * rstd, (bf_hi(v[m][j]) - mean) * rstd);
      const int ch = i8 + 8 * m;
      st128(dst + (ch >> 2) * 1024 + (ch & 3) * 256, o);
    }
  }
  TS(41); TFLUSH(0);
}

template <int OFF> DI void glds_o(const void* sbase, unsigned voff, unsigned laddr) {
  asm volatile("s_add_u32 m0, %2, %3\n\ts_nop 0\n\tglobal_load_lds_dwordx4 %0, %1" ::"v"(voff), "s"(sbase), "s"(laddr), "i"(OFF) : "scc");
}
template <int OFF> DI void glds_o48(const void* sbase, unsigned voff, unsigned laddr) {
  asm volatile("s_add_u32 m0, %2, %3\n\ts_mov_b32 exec_hi, 0xffff\n\tglobal_load_lds_dwordx4 %0, %1\n\ts_mov_b32 exec_hi, -1"
               ::"v"(voff), "s"(sbase), "s"(laddr), "i"(OFF) : "scc");
}

template <int RB, int NSEG, int NBS, int KS, int SK, int D, int LDW, bool PERM, bool SWAP>
struct Gemm {
  static constexpr int NST = KS / SK;
  static constexpr int NSLOT = D + 3;
  static constexpr int RS = 32 * NBS;
  static constexpr int PPK = 4 * RB + NSEG * NBS;
  static constexpr int STG = SK * PPK * 1024;
  static constexpr int LDS_BYTES = NSLOT * STG;
  static constexpr int NBI = NBS == 7 ? 2 : 1;
  static constexpr int CNT = SK * (RB + NSEG * NBI);
  static_assert(D + 1 < NST && D * CNT < 64 && (SK == 1 || SK == 2) && KS % 2 == 0, "cfg");

  template <class HK>
  static DI void load(int wl, unsigned lds0, const char* Ablk, const u16* W0, const u16* W1, int n0, int lane, HK&& hk TARG) {
    unsigned va[SK][RB], vb[SK][NBI];
#pragma unroll
    for (int i = 0; i < NBI; ++i) {
      const int e = wl * (NBS * 16) + i * 64 + lane;
      int pos, col;
      if constexpr (NBS == 7) {
        pos = (e * 4682) >> 16;
        col = (e - pos * 14) * 8;
        pos &= 31;
      } else {
        pos = e >> 3;
        const int slot = e & 7;
        col = 16 * ((slot >> 1) ^ ((pos >> 1) & 3)) + 8 * (slot & 1);
      }
      const int kk = PERM ? (8 * ((pos >> 2) & 3) + 4 * (pos >> 4) + (pos & 3)) : pos;
#pragma unroll
      for (int k = 0; k < SK; ++k) vb[k][i] = LDW ? ((k * 32 + kk) * LDW + n0 + col) * 2 : k * NBS * 1024 + e * 16;
    }
#pragma unroll
    for (int k = 0; k < SK; ++k)
#pragma unroll
      for (int r = 0; r < RB; ++r) va[k][r] = ((wl * RB + r) * KS + k) * 1024 + lane * 16;
    const unsigned mA = lds0 + wl * RB * 1024, mB = lds0 + 4 * RB * 1024 + wl * NBS * 256;
    const char* pa = Ablk;
    const char* p0 = (const char*)W0;
    const char* p1 = (const char*)W1;
    unsigned so = 0;
    auto issue = [&]() {
      const unsigned a = mA + so, b = mB + so;
      sfor<0, SK>([&](auto kk) {
        constexpr int k = decltype(kk)::v;
        sfor<0, RB>([&](auto rr) { glds_o<(k * PPK + decltype(rr)::v) * 1024>(pa, va[k][decltype(rr)::v], a); });
        glds_o<k * PPK * 1024>(p0, vb[k][0], b);
        if constexpr (NBI == 2) glds_o48<k * PPK * 1024 + 1024>(p0, vb[k][1], b);
        if constexpr (NSEG == 2) {
          glds_o<(k * PPK + NBS) * 1024>(p1, vb[k][0], b);
          if constexpr (NBI == 2) glds_o48<(k * PPK + NBS) * 1024 + 1024>(p1, vb[k][1], b);
        }
      });
      pa += SK * 1024;
      p0 += LDW ? SK * 32 * LDW * 2 : SK * NBS * 1024;
      p1 += LDW ? SK * 32 * LDW * 2 : SK * NBS * 1024;
      so += STG;
      so = so == NSLOT * STG ? 0 : so;
    };
#pragma unroll 1
    for (int T = -(D + 1); T < NST - D - 1; ++T) {
      if (T >= 0) wait_vm_bar<D * CNT>();
      issue();
      hk(T);
    }
    TS(1);
    sfor<0, D + 1>([&](auto tt) { wait_vm_bar<(D - decltype(tt)::v) * CNT>(); });
    bar();
  }

  template <int NW, int WR, class XR, class XF>
  static DI void compute(const lds_char* lds, f32x4 (&acc)[WR][NSEG * NW], int wid, int ns, int lane, XR&& xr, XF&& xf TARG) {
    constexpr int NBP = NBS == 7 ? 1 : NW;
    constexpr int NG = NSEG * NW;
    const unsigned ap0 = lds_addr(lds) + wid * WR * 1024 + lane * 16;
    unsigned bp0[NBP];
    if constexpr (NBS == 7) {
      bp0[0] = lds_addr(lds) + 4 * RB * 1024 + (lane >> 2) * 224 + 8 * (lane & 3) + ns * 32;
    } else {
      const unsigned tb = (lane >> 2) * 128 + (((lane >> 3) & 3) << 5) + 8 * (lane & 3);
#pragma unroll
      for (int g = 0; g < NW; ++g) bp0[g] = lds_addr(lds) + 4 * RB * 1024 + (tb ^ ((ns + g) << 5));
    }
    unsigned ap = ap0, bp[NBP];
#pragma unroll
    for (int g = 0; g < NBP; ++g) bp[g] = bp0[g];
    u32x4 a[2][WR];
    u32x4 b[SK][NG];
    auto rdA = [&](auto hh, auto kk) {
#pragma unroll
      for (int rb = 0; rb < WR; ++rb)
        a[decltype(hh)::v][rb] = lds_rd128((const lds_char*)(uintptr_t)ap + (decltype(kk)::v * PPK + rb) * 1024);
    };
    auto rdB = [&](auto kk, auto gg) {
      constexpr int k = decltype(kk)::v, g = decltype(gg)::v;
      constexpr int off = (k * PPK + (g / NW) * NBS) * 1024;
      const lds_char* p;
      if constexpr (NBS == 7) p = (const lds_char*)(uintptr_t)bp[0] + off + (g % NW) * 32;
      else p = (const lds_char*)(uintptr_t)bp[g % NW] + off;
      const u32x2 b0 = lds_tr(p), b1 = lds_tr(p + 16 * RS);
      b[k][g] = u32x4{b0[0], b0[1], b1[0], b1[1]};
    };
#pragma unroll
    for (int rb = 0; rb < WR; ++rb)
#pragma unroll
      for (int g = 0; g < NG; ++g) asm volatile("" : "+v"(acc[rb][g]));
    TS(1);
    bar();
    TS(2); TC(50);
    sfor<0, SK>([&](auto kk) {
      rdA(kk, kk);
      if constexpr (SK == 1) xr(IC<2>{});
      sfor<0, NG>([&](auto gg) { rdB(kk, gg); });
    });
    __builtin_amdgcn_s_waitcnt(0xc07f);
    sfor<0, NG * WR>([&](auto gg) { xf(gg, IC<1>{}, a[0]); });
    unsigned so = 0;
#pragma unroll 1
    for (int it = 0; it < KS / 2; ++it) {
      sfor<0, 2>([&](auto hh) {
        constexpr int h = decltype(hh)::v;
        constexpr int kb = SK == 1 ? 0 : h;
        if constexpr (SK == 1 || h == 0) {
          so += STG;
          so = so == NSLOT * STG ? 0 : so;
          ap = ap0 + so;
#pragma unroll
          for (int g = 0; g < NBP; ++g) bp[g] = bp0[g] + so;
          bar();
        }
        if constexpr (SK == 1) {
          rdA(IC<h ^ 1>{}, IC<0>{});
          xr(hh);
        }
        sched_fence();
        sfor<0, NG>([&](auto gg) {
          constexpr int g = decltype(gg)::v;
          sfor<0, WR>([&](auto rr) {
            constexpr int rb = decltype(rr)::v;
            acc[rb][g] = SWAP ? mfma16(b[kb][g], a[h][rb], acc[rb][g]) : mfma16(a[h][rb], b[kb][g], acc[rb][g]);
            if constexpr (rb < WR - 1) {
              xf(IC<g * WR + rb>{}, hh, a[h ^ 1]);
              sched_fence();
            }
          });
          rdB(IC<kb>{}, gg);
          if constexpr (SK == 2 && g == NG - 1) rdA(hh, hh);
          xf(IC<g * WR + WR - 1>{}, hh, a[h ^ 1]);
          sched_fence();
        });
      });
    }
  }
};

struct NoX {
  template <class... A> DI void operator()(A&&...) const {}
};

constexpr int D1 = 4;
#define XF0 6
typedef Gemm<2, 1, 7, 14, 1, D1, 1344, true, true> G1S;

template <bool SWAP, int NW>
DI void k1_compute(const lds_char* lds, char* __restrict__ ws, int mt, int nt, int wid, int lane TARG) {
  constexpr int ns = 0;
  typedef Gemm<2, 1, 7, 14, 1, D1, 1344, true, SWAP> G;
  const lds_char* vec = lds + G::LDS_BYTES;
  f32x4 acc[2][NW];
#pragma unroll
  for (int rb = 0; rb < 2; ++rb)
#pragma unroll
    for (int nb = 0; nb < NW; ++nb) acc[rb][nb] = f32x4{0.f, 0.f, 0.f, 0.f};
  const int gq = lane >> 4, t = lane & 15;
  const lds_char* vp = vec + gq * 32;
  f32x4 sv[2], mv[2];
  G::template compute<NW, 2>(
      lds, acc, wid, ns, lane,
      [&](auto hh) {
        if constexpr (decltype(hh)::v == 2) {
          sv[0] = lds_rd128f(vp);
          sv[1] = lds_rd128f(vp + 16);
          mv[0] = lds_rd128f(vp + 1792);
          mv[1] = lds_rd128f(vp + 1792 + 16);
          vp += 128;
        }
      },
      [&](auto gg, auto, u32x4(&a)[2]) {
        constexpr int g = decltype(gg)::v - XF0;
        if constexpr (g >= 0 && g < 8) {
          constexpr int j = g >> 1, rb = g & 1;
          a[rb][j] = pk_bf16(mix_lo(a[rb][j], mv[j >> 1][(j & 1) * 2], sv[j >> 1][(j & 1) * 2]),
                             mix_hi(a[rb][j], mv[j >> 1][(j & 1) * 2 + 1], sv[j >> 1][(j & 1) * 2 + 1]));
          if constexpr ((g & 3) == 3) {
            sv[g >> 2] = lds_rd128f(vp + (g >> 2) * 16);
            mv[g >> 2] = lds_rd128f(vp + 1792 + (g >> 2) * 16);
            if constexpr (g == 7) vp += 128;
          }
        }
      } TPASS);
  TS(40); TC(51);
  const int sec = nt >> 2, h0 = (nt & 3) * 4;
  if constexpr (SWAP) {
    const float sc = sec == 0 ? 0.18898223650461362f * 1.4426950408889634f : 1.f;
    char* base = ws + (sec == 0 ? WS_Q : WS_K);
#pragma unroll
    for (int rb = 0; rb < 2; ++rb) {
      const int tb = mt * 8 + wid * 2 + rb;
#pragma unroll
      for (int nb = 0; nb < NW; ++nb) {
        const int Gi = 4 * (ns + nb) + gq;
        const int hd = (Gi * 37) >> 8, dg = Gi - 7 * hd;
        char* dst = base + ((size_t)(h0 + hd) * 128 + tb) * 1024 + ((dg >> 1) * 16 + t) * 16 + (dg & 1) * 8;
        const f32x4 v = acc[rb][nb];
        const unsigned lo = pk_bf16(v[0] * sc, v[1] * sc), hi = pk_bf16(v[2] * sc, v[3] * sc);
        if (dg == 6) st128(dst, u32x4{lo, hi, 0u, 0u});
        else st64(dst, u32x2{lo, hi});
      }
    }
  } else {
    const int s32 = mt * 4 + wid;
    char* base = ws + WS_V;
#pragma unroll
    for (int nb = 0; nb < NW; ++nb) {
      const int n = 16 * (ns + nb) + t;
      const int hd = (n * 147) >> 12, d = n - 28 * hd;
      char* dst = base + (((size_t)(h0 + hd) * 64 + s32) * 2 + (d >> 4)) * 1024 + (gq * 16 + (d & 15)) * 16;
      const f32x4 v0 = acc[0][nb], v1 = acc[1][nb];
      st128(dst, u32x4{pk_bf16(v0[0], v0[1]), pk_bf16(v0[2], v0[3]), pk_bf16(v1[0], v1[1]), pk_bf16(v1[2], v1[3])});
    }
    if (lane < 16) {
      char* dst = base + (((size_t)(h0 + (lane >> 2)) * 64 + s32) * 2 + 1) * 1024 + ((lane & 3) * 16 + 12) * 16;
      st128(dst, u32x4{0x3f803f80u, 0x3f803f80u, 0x3f803f80u, 0x3f803f80u});
    }
  }
}

__global__ __launch_bounds__(512, 1) void k1_qkv(const u16* __restrict__ wqkv, const u16* __restrict__ wg,
                                                 const u16* __restrict__ wu, char* __restrict__ ws) {
  TDECL; TS(0);
  extern __shared__ __attribute__((aligned(16))) char smem[];
  lds_char* lds = (lds_char*)smem;
  const int lane = threadIdx.x & 63, wid = __builtin_amdgcn_readfirstlane(threadIdx.x >> 6);
  const int bid = blockIdx.x, xcd = bid & 7, j = bid >> 3;
  if (j >= 24) {
    const int e = (j - 24) * 8 + xcd;
    const int mat = e >> 5, c0 = (e & 31) * 56;
    const u16* W = mat ? wu : wg;
    const int tid = threadIdx.x;
    float* red = (float*)smem;
    if (tid < 448) {
      const int rg = tid / 7, cq = tid - rg * 7;
      u32x4 w[7];
      float m[7], sb[7];
#pragma unroll
      for (int i = 0; i < 7; ++i) {
        const int k = rg + 64 * i;
        w[i] = *(const u32x4*)(W + (size_t)k * 1792 + c0 + cq * 8);
        sb[i] = ((const float*)(ws + WS_MOD))[3 * 448 + k];
        m[i] = ((const float*)(ws + WS_MOD))[4 * 448 + k];
      }
      float a1[8], a2[8];
#pragma unroll
      for (int q = 0; q < 8; ++q) a1[q] = a2[q] = 0.f;
#pragma unroll
      for (int i = 0; i < 7; ++i)
#pragma unroll
        for (int q = 0; q < 4; ++q) {
          a1[2 * q] += m[i] * bf_lo(w[i][q]);
          a1[2 * q + 1] += m[i] * bf_hi(w[i][q]);
          a2[2 * q] += sb[i] * bf_lo(w[i][q]);
          a2[2 * q + 1] += sb[i] * bf_hi(w[i][q]);
        }
#pragma unroll
      for (int q = 0; q < 8; ++q) {
        red[rg * 113 + cq * 8 + q] = a1[q];
        red[rg * 113 + 56 + cq * 8 + q] = a2[q];
      }
    }
    __syncthreads();
    if (tid < 448) {
      const int col = tid >> 2, part = tid & 3;
      float s = 0.f;
#pragma unroll
      for (int i = 0; i < 16; ++i) s += red[(part * 16 + i) * 113 + col];
      s += __shfl_xor(s, 1);
      s += __shfl_xor(s, 2);
      if (part == 0) {
        const int which = col >= 56, n = c0 + col - which * 56;
        const int nt = n / 112;
        ((float*)(ws + WS_U))[(nt * 4 + mat * 2 + which) * 112 + n - nt * 112] = s;
      }
    }
    TS(41); TFLUSH(1);
    return;
  }
  const int mt = (xcd >> 1) * 4 + (j & 3), nt = (xcd & 1) * 6 + (j >> 2);
  if (wid < 4) {
    const int off = wid * 1024 + lane * 16;
    glds(ws + WS_MOD, off < 3584 ? off : 0, lds_addr(lds) + G1S::LDS_BYTES + wid * 1024);
    G1S::load(wid, lds_addr(lds), ws + WS_XN + (size_t)mt * 8 * 14 * 1024, wqkv, wqkv, nt * 112, lane, NoX{} TPASS);
  } else {
    if (nt < 8) k1_compute<true, 7>(lds, ws, mt, nt, wid - 4, lane TPASS);
    else k1_compute<false, 7>(lds, ws, mt, nt, wid - 4, lane TPASS);
  }
  TS(41); TFLUSH(1);
}

DI void k2_wave(const lds_char* lds, char* __restrict__ ws, int qt, int head, int wid, int lane) {
  const int gq = lane >> 4, t = lane & 15;
  const int lp0 = wid >> 1;
  const int qb = 8 * qt + wid;
  const lds_char* kp = lds + lp0 * 2048 + lane * 16;
  const lds_char* vp = lds + 24576 + lp0 * 2048 + lane * 16;
  const u32x4 qf = lds_rd128(lds + 49152 + wid * 1024 + lane * 16);
  f32x4 sc[18];
  const float NEG = -__builtin_inff();
#pragma unroll
  for (int i = 0; i < 18; ++i) {
    const u32x4 kf = lds_rd128(kp + i * 1024);
    sc[i] = mfma16(kf, qf, f32x4{0.f, 0.f, 0.f, 0.f});
  }
  const int d0 = 256 + 16 * (wid & 1) + t - 4 * gq;
#pragma unroll
  for (int r = 0; r < 4; ++r) {
    if (d0 - r >= 256) sc[0][r] = NEG;
    if (d0 - 16 - r >= 256) sc[1][r] = NEG;
    if (d0 - 256 - r < 0) sc[16][r] = NEG;
    if (d0 - 272 - r < 0) sc[17][r] = NEG;
  }
  if (qt < 2) {
    const int kb0 = 8 * qt - 16 + 2 * lp0;
#pragma unroll
    for (int i = 0; i < 16; ++i)
      if (kb0 + i < 0) sc[i] = f32x4{NEG, NEG, NEG, NEG};
  }
  float m = sc[16][0];
#pragma unroll
  for (int i = 0; i < 18; ++i) m = fmaxf(fmaxf(m, fmaxf(sc[i][0], sc[i][1])), fmaxf(sc[i][2], sc[i][3]));
  m = fmaxf(m, __shfl_xor(m, 16));
  m = fmaxf(m, __shfl_xor(m, 32));
  unsigned pp[18][2];
#pragma unroll
  for (int i = 0; i < 18; ++i) {
    pp[i][0] = pk_bf16(__builtin_amdgcn_exp2f(sc[i][0] - m), __builtin_amdgcn_exp2f(sc[i][1] - m));
    pp[i][1] = pk_bf16(__builtin_amdgcn_exp2f(sc[i][2] - m), __builtin_amdgcn_exp2f(sc[i][3] - m));
  }
  wait_vm_bar<0>();
  f32x4 o0 = {0.f, 0.f, 0.f, 0.f}, o1 = {0.f, 0.f, 0.f, 0.f};
#pragma unroll
  for (int p = 0; p < 9; ++p) {
    const u32x4 pf = {pp[2 * p][0], pp[2 * p][1], pp[2 * p + 1][0], pp[2 * p + 1][1]};
    const u32x4 v0 = lds_rd128(vp + p * 2048), v1 = lds_rd128(vp + p * 2048 + 1024);
    o0 = mfma16(v0, pf, o0);
    o1 = mfma16(v1, pf, o1);
  }
  const float sum = __shfl(o1[0], 48 + t);
  const float inv = __builtin_amdgcn_rcpf(sum);
  char* base = ws + WS_O + (size_t)qb * 14 * 1024;
  {
    const int Gi = 7 * head + gq;
    st64(base + (Gi >> 3) * 1024 + (((Gi >> 1) & 3) * 16 + t) * 16 + (Gi & 1) * 8, u32x2{pk_bf16(o0[0] * inv, o0[1] * inv), pk_bf16(o0[2] * inv, o0[3] * inv)});
  }
  if (gq < 3) {
    const int Gi = 7 * head + 4 + gq;
    st64(base + (Gi >> 3) * 1024 + (((Gi >> 1) & 3) * 16 + t) * 16 + (Gi & 1) * 8, u32x2{pk_bf16(o1[0] * inv, o1[1] * inv), pk_bf16(o1[2] * inv, o1[3] * inv)});
  }
}

__global__ __launch_bounds__(512, 2) void k2_attn(char* __restrict__ ws) {
  TDECL; TS(0);
  extern __shared__ __attribute__((aligned(16))) char smem[];
  lds_char* lds = (lds_char*)smem;
  const int lane = threadIdx.x & 63, wid = __builtin_amdgcn_readfirstlane(threadIdx.x >> 6);
  const int bid = blockIdx.x, xcd = bid & 7, j = bid >> 3;
  const int head = xcd * 2 + (j & 1), qt = j >> 1;
  const unsigned l0 = lds_addr(lds);
  glds(ws + WS_Q + ((size_t)head * 128 + 8 * qt + wid) * 1024, lane * 16, l0 + 49152 + wid * 1024);
#pragma unroll
  for (int i = 0; i < 3; ++i) {
    const int lk = wid * 3 + i;
    int tb = 8 * qt - 16 + lk;
    tb = tb < 0 ? 0 : tb;
    glds(ws + WS_K + ((size_t)head * 128 + tb) * 1024, lane * 16, l0 + lk * 1024);
  }
#pragma unroll
  for (int i = 0; i < 3; ++i) {
    const int lv = wid * 3 + i;
    int f = (4 * qt - 8) * 2 + lv;
    f = f < 0 ? (lv & 1) : f;
    glds(ws + WS_V + ((size_t)head * 128 + f) * 1024, lane * 16, l0 + 24576 + lv * 1024);
  }
  TS(1);
  wait_vm_bar<3>();
  TS(2);
  k2_wave(lds, ws, qt, head, wid, lane);
  TS(41); TFLUSH(2);
}

typedef Gemm<1, 1, 4, 14, 2, 4, 448, true, true> G3;
__global__ __launch_bounds__(512, 1) void k3_wo(const u16* __restrict__ x, const u16* __restrict__ wo, char* __restrict__ ws) {
  TDECL; TS(0);
  extern __shared__ __attribute__((aligned(16))) char smem[];
  lds_char* lds = (lds_char*)smem;
  const int lane = threadIdx.x & 63, wid = __builtin_amdgcn_readfirstlane(threadIdx.x >> 6);
  const int bid = blockIdx.x, xcd = bid & 7, j = bid >> 3;
  const int mt = xcd * 4 + (j & 3), nt = j >> 2;
  if (wid >= 4) {
    G3::load(wid - 4, lds_addr(lds), ws + WS_O + (size_t)mt * 4 * 14 * 1024, wo, wo, nt * 64, lane, NoX{} TPASS);
    TS(41); TFLUSH(3);
    return;
  }
  const int gq = lane >> 4, t = lane & 15;
  const int tb = mt * 4 + wid, row = tb * 16 + t;
  u32x2 xr[4];
  f32x4 ga[4], m2[4];
#pragma unroll
  for (int nb = 0; nb < 4; ++nb) {
    xr[nb] = *(const u32x2*)(x + (size_t)row * 448 + nt * 64 + nb * 16 + gq * 4);
    ga[nb] = *(const f32x4*)((const float*)(ws + WS_MOD) + 2 * 448 + nt * 64 + nb * 16 + gq * 4);
    m2[nb] = *(const f32x4*)((const float*)(ws + WS_MOD) + 4 * 448 + nt * 64 + nb * 16 + gq * 4);
  }
  f32x4 acc[1][4];
#pragma unroll
  for (int nb = 0; nb < 4; ++nb) acc[0][nb] = f32x4{0.f, 0.f, 0.f, 0.f};
  G3::compute<4, 1>(lds, acc, wid, 0, lane, NoX{}, NoX{} TPASS);
  TS(40); TC(51);
  unsigned o[4][2], om[4][2];
  float v[4][4];
  float s = 0.f;
#pragma unroll
  for (int nb = 0; nb < 4; ++nb) {
    o[nb][0] = pk_bf16(bf_lo(xr[nb][0]) + ga[nb][0] * acc[0][nb][0], bf_hi(xr[nb][0]) + ga[nb][1] * acc[0][nb][1]);
    o[nb][1] = pk_bf16(bf_lo(xr[nb][1]) + ga[nb][2] * acc[0][nb][2], bf_hi(xr[nb][1]) + ga[nb][3] * acc[0][nb][3]);
    v[nb][0] = bf_lo(o[nb][0]); v[nb][1] = bf_hi(o[nb][0]); v[nb][2] = bf_lo(o[nb][1]); v[nb][3] = bf_hi(o[nb][1]);
    om[nb][0] = pk_bf16(v[nb][0] * m2[nb][0], v[nb][1] * m2[nb][1]);
    om[nb][1] = pk_bf16(v[nb][2] * m2[nb][2], v[nb][3] * m2[nb][3]);
    s += (v[nb][0] + v[nb][1]) + (v[nb][2] + v[nb][3]);
  }
  char* dst = ws + WS_X1 + ((size_t)tb * 14 + nt * 2) * 1024 + lane * 16;
  st128(dst + (WS_XM - WS_X1), u32x4{om[0][0], om[0][1], om[1][0], om[1][1]});
  st128(dst + (WS_XM - WS_X1) + 1024, u32x4{om[2][0], om[2][1], om[3][0], om[3][1]});
  st128(dst, u32x4{o[0][0], o[0][1], o[1][0], o[1][1]});
  st128(dst + 1024, u32x4{o[2][0], o[2][1], o[3][0], o[3][1]});
  s += __shfl_xor(s, 16);
  s += __shfl_xor(s, 32);
  const float mc = s * (1.f / 64.f);
  float q = 0.f;
#pragma unroll
  for (int nb = 0; nb < 4; ++nb)
#pragma unroll
    for (int r = 0; r < 4; ++r) q += (v[nb][r] - mc) * (v[nb][r] - mc);
  q += __shfl_xor(q, 16);
  q += __shfl_xor(q, 32);
  if (gq == 0) *(f32x2*)(ws + WS_ST + ((size_t)row * 7 + nt) * 8) = f32x2{mc, q};
  TS(41); TFLUSH(3);
}

typedef Gemm<2, 2, 7, 14, 1, 3, 1792, false, true> G4;
template <int NW>
DI void k4_compute(const lds_char* lds, char* __restrict__ ws, int mt, int nt, int wid, int lane TARG) {
  const int odd = nt & 1;
  const int ns = NW == 4 ? 3 * odd : 4 - 4 * odd;
  const int gq = lane >> 4, t = lane & 15;
  f32x4 acc[2][2 * NW];
#pragma unroll
  for (int rb = 0; rb < 2; ++rb)
#pragma unroll
    for (int nb = 0; nb < 2 * NW; ++nb) acc[rb][nb] = f32x4{0.f, 0.f, 0.f, 0.f};
  G4::compute<NW, 2>(lds, acc, wid, ns, lane, NoX{}, NoX{} TPASS);
  TS(40); TC(51);
  const lds_char* ex = lds + G4::LDS_BYTES;
  const lds_char* up = ex + gq * 16 + ns * 64;
  const int B0 = nt * 7 + ns;
  typedef const __attribute__((address_space(3))) f32x2* lp2;
  const f32x2 st0 = *(lp2)(ex + 9216 + (wid * 32 + t) * 8), st1 = *(lp2)(ex + 9216 + (wid * 32 + 16 + t) * 8);
  char* base = ws + WS_HH + (size_t)(mt * 8 + wid * 2) * 56 * 1024 + lane * 16 + (B0 >> 1) * 1024;
  unsigned o[2][NW][2];
#pragma unroll
  for (int nb = 0; nb < NW; ++nb) {
    const f32x4 u1g = lds_rd128f(up + nb * 64), u2g = lds_rd128f(up + nb * 64 + 448);
    const f32x4 u1u = lds_rd128f(up + nb * 64 + 896), u2u = lds_rd128f(up + nb * 64 + 1344);
#pragma unroll
    for (int rb = 0; rb < 2; ++rb) {
      const f32x2 st = rb ? st1 : st0;
      const f32x2 nm = {st[0], st[0]}, rs2 = {st[1], st[1]};
#pragma unroll
      for (int r = 0; r < 2; ++r) {
        const f32x2 ag = {acc[rb][nb][2 * r], acc[rb][nb][2 * r + 1]}, au = {acc[rb][NW + nb][2 * r], acc[rb][NW + nb][2 * r + 1]};
        const f32x2 gv = rs2 * ag + (nm * f32x2{u1g[2 * r], u1g[2 * r + 1]} + f32x2{u2g[2 * r], u2g[2 * r + 1]});
        const f32x2 uv = rs2 * au + (nm * f32x2{u1u[2 * r], u1u[2 * r + 1]} + f32x2{u2u[2 * r], u2u[2 * r + 1]});
        const f32x2 z = gv * (gv * gv * f32x2{-0.1029432397f, -0.1029432397f} + f32x2{-2.302208198f, -2.302208198f});
        const f32x2 d = f32x2{__builtin_amdgcn_exp2f(z[0]), __builtin_amdgcn_exp2f(z[1])} + f32x2{1.f, 1.f};
        const f32x2 h = gv * uv * f32x2{__builtin_amdgcn_rcpf(d[0]), __builtin_amdgcn_rcpf(d[1])};
        o[rb][nb][r] = pk_bf16(h[0], h[1]);
      }
      if (NW == 4 && (nb & 1))
        st128(base + rb * 56 * 1024 + (nb >> 1) * 1024, u32x4{o[rb][nb - 1][0], o[rb][nb - 1][1], o[rb][nb][0], o[rb][nb][1]});
    }
  }
  if constexpr (NW == 3) {
#pragma unroll
    for (int rb = 0; rb < 2; ++rb) {
      char* bs = base + rb * 56 * 1024;
      if (odd) {
        st64(bs + 8, u32x2{o[rb][0][0], o[rb][0][1]});
        st128(bs + 1024, u32x4{o[rb][1][0], o[rb][1][1], o[rb][2][0], o[rb][2][1]});
      } else {
        st128(bs, u32x4{o[rb][0][0], o[rb][0][1], o[rb][1][0], o[rb][1][1]});
        st64(bs + 1024, u32x2{o[rb][2][0], o[rb][2][1]});
      }
    }
  }
}

__global__ __launch_bounds__(768, 3) void k4_gateup(const u16* __restrict__ wg, const u16* __restrict__ wu, char* __restrict__ ws) {
  TDECL; TS(0);
  extern __shared__ __attribute__((aligned(16))) char smem[];
  lds_char* lds = (lds_char*)smem;
  const int lane = threadIdx.x & 63, wid = __builtin_amdgcn_readfirstlane(threadIdx.x >> 6);
  const int bid = blockIdx.x, xcd = bid & 7, j = bid >> 3;
  const int mt = (xcd >> 2) * 8 + (j & 7), nt = (xcd & 3) * 4 + (j >> 3);
  if (wid < 4) {
    const int wl = wid;
    const unsigned ex = lds_addr(lds) + G4::LDS_BYTES;
    if (wl < 2) {
      const int off = wl * 1024 + lane * 16;
      glds(ws + WS_U + (size_t)nt * 1792, off < 1792 ? off : 0, ex + wl * 1024);
    }
#pragma unroll
    for (int i = 0; i < 2; ++i) {
      const int o2 = (wl * 2 + i) * 1024 + lane * 16;
      glds(ws + WS_ST + (size_t)mt * 7168, o2 < 7168 ? o2 : 0, ex + 2048 + (wl * 2 + i) * 1024);
    }
    float mc[7], q = 0.f, mean = 0.f;
#pragma unroll
    for (int c = 0; c < 7; ++c) mc[c] = 0.f;
    const int row = wl * 32 + (lane & 31);
    G4::load(wl, lds_addr(lds), ws + WS_XM + (size_t)mt * 8 * 14 * 1024, wg, wu, nt * 112, lane, [&](int T) {
      if (T == 1) {
        const lds_char* sp = lds + G4::LDS_BYTES + 2048 + row * 56;
        float sm = 0.f;
#pragma unroll
        for (int c = 0; c < 7; ++c) {
          const f32x2 e = *(const __attribute__((address_space(3))) f32x2*)(sp + c * 8);
          mc[c] = e[0]; q += e[1]; sm += e[0];
        }
        mean = sm * (1.f / 7.f);
      }
      if (T == 3) {
#pragma unroll
        for (int c = 0; c < 7; ++c) q += 64.f * (mc[c] - mean) * (mc[c] - mean);
        const float rs = rsqrtf(q * (1.f / 448.f) + 1e-5f);
        *(__attribute__((address_space(3))) f32x2*)(lds + G4::LDS_BYTES + 9216 + row * 8) = f32x2{-mean * rs, rs};
        asm volatile("s_waitcnt lgkmcnt(0)" ::: "memory");
      }
    } TPASS);
  } else if (wid < 8) {
    k4_compute<4>(lds, ws, mt, nt, wid - 4, lane TPASS);
  } else {
    k4_compute<3>(lds, ws, mt, nt, wid - 8, lane TPASS);
  }
  TS(41); TFLUSH(4);
}

typedef Gemm<1, 1, 4, 56, 2, 4, 448, false, true> G5;
__global__ __launch_bounds__(512, 1) void k5_down(const u16* __restrict__ wd, char* __restrict__ ws, u16* __restrict__ out) {
  TDECL; TS(0);
  extern __shared__ __attribute__((aligned(16))) char smem[];
  lds_char* lds = (lds_char*)smem;
  const int lane = threadIdx.x & 63, wid = __builtin_amdgcn_readfirstlane(threadIdx.x >> 6);
  const int bid = blockIdx.x, xcd = bid & 7, j = bid >> 3;
  const int mt = xcd * 4 + (j & 3), nt = j >> 2;
  if (wid >= 4) {
    G5::load(wid - 4, lds_addr(lds), ws + WS_HH + (size_t)mt * 4 * 56 * 1024, wd, wd, nt * 64, lane, NoX{} TPASS);
    TS(41); TFLUSH(5);
    return;
  }
  const int gq = lane >> 4, t = lane & 15, wr = wid >> 1, wc = wid & 1;
  const int tb = mt * 4 + wr * 2;
  u32x4 xr[2];
  f32x4 gb[2];
#pragma unroll
  for (int i = 0; i < 2; ++i) xr[i] = *(const u32x4*)(ws + WS_X1 + ((size_t)(tb + i) * 14 + nt * 2 + wc) * 1024 + lane * 16);
#pragma unroll
  for (int nb = 0; nb < 2; ++nb) gb[nb] = *(const f32x4*)((const float*)(ws + WS_MOD) + 5 * 448 + nt * 64 + wc * 32 + nb * 16 + gq * 4);
  f32x4 acc[2][2];
#pragma unroll
  for (int nb = 0; nb < 2; ++nb) acc[0][nb] = acc[1][nb] = f32x4{0.f, 0.f, 0.f, 0.f};
  G5::compute<2, 2>(lds, acc, wr, wc * 2, lane, NoX{}, NoX{} TPASS);
  TS(40); TC(51);
#pragma unroll
  for (int rb = 0; rb < 2; ++rb) {
    unsigned lo[2], hi[2];
#pragma unroll
    for (int nb = 0; nb < 2; ++nb) {
      const unsigned x0 = xr[rb][nb * 2], x1 = xr[rb][nb * 2 + 1];
      lo[nb] = pk_bf16(bf_lo(x0) + gb[nb][0] * acc[rb][nb][0], bf_hi(x0) + gb[nb][1] * acc[rb][nb][1]);
      hi[nb] = pk_bf16(bf_lo(x1) + gb[nb][2] * acc[rb][nb][2], bf_hi(x1) + gb[nb][3] * acc[rb][nb][3]);
    }
    const auto l = __builtin_amdgcn_permlane16_swap(lo[0], lo[1], false, false);
    const auto h = __builtin_amdgcn_permlane16_swap(hi[0], hi[1], false, false);
    u16* dst = out + (size_t)((tb + rb) * 16 + t) * 448 + nt * 64 + wc * 32 + (gq & 1) * 16 + (gq >> 1) * 8;
    st128(dst, u32x4{l[0], h[0], l[1], h[1]});
  }
  TS(41); TFLUSH(5);
}

extern "C" void kernel_launch(void* const* d_in, const int* in_sizes, int n_in, void* d_out, int out_size, void* d_ws,
                              size_t ws_size, hipStream_t stream) {
  const u16* x = (const u16*)d_in[0];
  const u16* wqkv = (const u16*)d_in[1];
  const u16* wo = (const u16*)d_in[2];
  const u16* wg = (const u16*)d_in[4];
  const u16* wu = (const u16*)d_in[5];
  const u16* wd = (const u16*)d_in[6];
  const u16* c = (const u16*)d_in[7];
  const u16* wmod = (const u16*)d_in[8];
  char* ws = (char*)d_ws;
  constexpr int L1 = G1S::LDS_BYTES + 4096;
  constexpr int L2 = 49152 + 8192;
  constexpr int L3 = G3::LDS_BYTES;
  constexpr int L4 = G4::LDS_BYTES + 2048 + 8192;
  constexpr int L5 = G5::LDS_BYTES;
  static_assert(L1 <= 163840 && L3 <= 163840 && L4 <= 163840 && L5 <= 163840, "lds");
  (void)hipFuncSetAttribute((const void*)k1_qkv, hipFuncAttributeMaxDynamicSharedMemorySize, L1);
  (void)hipFuncSetAttribute((const void*)k3_wo, hipFuncAttributeMaxDynamicSharedMemorySize, L3);
  (void)hipFuncSetAttribute((const void*)k4_gateup, hipFuncAttributeMaxDynamicSharedMemorySize, L4);
  (void)hipFuncSetAttribute((const void*)k5_down, hipFuncAttributeMaxDynamicSharedMemorySize, L5);
  k0_mod_ln<<<148, 256, 0, stream>>>(x, c, wmod, ws);
  k1_qkv<<<256, 512, L1, stream>>>(wqkv, wg, wu, ws);
  k2_attn<<<256, 512, L2, stream>>>(ws);
  k3_wo<<<224, 512, L3, stream>>>(x, wo, ws);
  k4_gateup<<<256, 768, L4, stream>>>(wg, wu, ws);
  k5_down<<<224, 512, L5, stream>>>(wd, ws, (u16*)d_out);
}
```

```cpp
#include <hip/hip_runtime.h>
#include <hip/hip_bf16.h>
#include <stdint.h>

typedef __attribute__((ext_vector_type(8))) __bf16 bf16x8;
typedef __attribute__((ext_vector_type(2))) __bf16 bf16x2;
typedef __attribute__((ext_vector_type(2))) _Float16 f16x2;
typedef __attribute__((ext_vector_type(4))) float f32x4;
typedef __attribute__((ext_vector_type(2))) float f32x2;
typedef __attribute__((ext_vector_type(4))) short s16x4;
typedef __attribute__((ext_vector_type(4))) unsigned u32x4;
typedef __attribute__((ext_vector_type(2))) unsigned u32x2;
typedef __attribute__((address_space(3))) char lds_char;
typedef unsigned short u16;

#define DI __device__ __forceinline__
#define TDECL
#define TS(i)
#define TC(i)
#define TFLUSH(k)
#define TARG
#define TPASS

constexpr size_t WS_MOD = 0;
constexpr size_t WS_U = 16384;
constexpr size_t WS_XN = 65536;
constexpr size_t WS_Q = WS_XN + 128 * 14 * 1024;
constexpr size_t WS_K = WS_Q + 16 * 128 * 1024;
constexpr size_t WS_V = WS_K + 16 * 128 * 1024;
constexpr size_t WS_O = WS_V + 16 * 128 * 1024;
constexpr size_t WS_X1 = WS_O + 128 * 14 * 1024;
constexpr size_t WS_XM = WS_X1 + 128 * 14 * 1024;
constexpr size_t WS_ST = WS_XM + 128 * 14 * 1024;
constexpr size_t WS_HH = WS_ST + 2048 * 7 * 8;

template <int N> struct IC { static constexpr int v = N; };
template <int I, int N, class F> DI void sfor(F&& f) {
  if constexpr (I < N) { f(IC<I>{}); sfor<I + 1, N>(f); }
}
template <int N> DI void wait_vm_bar() { asm volatile("s_waitcnt vmcnt(%0)\n\ts_barrier" ::"i"(N) : "memory"); }
DI void bar() { asm volatile("s_barrier" ::: "memory"); }
DI void sched_fence() { __builtin_amdgcn_sched_barrier(0); }

DI float bf_lo(unsigned u) { return __uint_as_float(u << 16); }
DI float bf_hi(unsigned u) { return __uint_as_float(u & 0xffff0000u); }
DI unsigned pk_bf16(float lo, float hi) {
  return __builtin_bit_cast(unsigned, __builtin_convertvector(f32x2{lo, hi}, bf16x2));
}
DI unsigned pk_f16(float lo, float hi) {
  return __builtin_bit_cast(unsigned, __builtin_convertvector(f32x2{lo, hi}, f16x2));
}
DI float h_lo(unsigned u) { return (float)__builtin_bit_cast(f16x2, u)[0]; }
DI float h_hi(unsigned u) { return (float)__builtin_bit_cast(f16x2, u)[1]; }
DI float mix_lo(unsigned h, float m, float a) {
  float r;
  asm("v_fma_mix_f32 %0, %1, %2, %3 op_sel_hi:[1,0,0]" : "=v"(r) : "v"(h), "v"(m), "v"(a));
  return r;
}
DI float mix_hi(unsigned h, float m, float a) {
  float r;
  asm("v_fma_mix_f32 %0, %1, %2, %3 op_sel:[1,0,0] op_sel_hi:[1,0,0]" : "=v"(r) : "v"(h), "v"(m), "v"(a));
  return r;
}
DI float bf_round(float x) { return bf_lo(pk_bf16(x, 0.f)); }
DI float bf2f(u16 v) { return __uint_as_float(((unsigned)v) << 16); }

DI void glds(const void* sbase, unsigned voff, unsigned laddr) {
  asm volatile("s_mov_b32 m0, %2\n\ts_nop 0\n\tglobal_load_lds_dwordx4 %0, %1" ::"v"(voff), "s"(sbase), "s"(laddr));
}
DI void pf(unsigned& d, const void* p) {}
DI void pf_done(unsigned d) { asm volatile("s_waitcnt vmcnt(0)" ::"v"(d)); }
#define STP "sc1"
DI void st128(void* p, u32x4 v) { asm volatile("global_store_dwordx4 %0, %1, off " STP "\n\ts_nop 1" ::"v"(p), "v"(v) : "memory"); }
DI void st64(void* p, u32x2 v) { asm volatile("global_store_dwordx2 %0, %1, off " STP "\n\ts_nop 1" ::"v"(p), "v"(v) : "memory"); }
DI unsigned lds_addr(const lds_char* p) { return (unsigned)(uintptr_t)p; }
DI u32x4 lds_rd128(const lds_char* p) { return *(const __attribute__((address_space(3))) u32x4*)p; }
DI f32x4 lds_rd128f(const lds_char* p) { return *(const __attribute__((address_space(3))) f32x4*)p; }
DI u32x2 lds_tr(const lds_char* p) {
  return __builtin_bit_cast(u32x2, __builtin_amdgcn_ds_read_tr16_b64_v4i16((__attribute__((address_space(3))) s16x4*)p));
}
DI f32x4 mfma16(u32x4 a, u32x4 b, f32x4 c) {
  return __builtin_amdgcn_mfma_f32_16x16x32_bf16(__builtin_bit_cast(bf16x8, a), __builtin_bit_cast(bf16x8, b), c, 0, 0, 0);
}

#define TOUCH_CODE(sym, N)                                                                                         \
  {                                                                                                                \
    const char* p_;                                                                                                \
    asm volatile("s_getpc_b64 s[20:21]\n\ts_add_u32 s20, s20, " sym "@rel32@lo+4\n\ts_addc_u32 s21, s21, " sym     \
                 "@rel32@hi+12\n\ts_mov_b64 %0, s[20:21]" : "=s"(p_) : : "s20", "s21", "scc");                     \
    _Pragma("unroll") for (int o_ = 0; o_ < N; o_ += 4096) {                                                       \
      const int f_ = o_ + tid * 16;                                                                                \
      acc_ ^= *(const u32x4*)(p_ + (f_ < N - 16 ? f_ : N - 16));                                                   \
    }                                                                                                              \
  }
__global__ __launch_bounds__(256) void k0_mod_ln(const u16* __restrict__ x, const u16* __restrict__ c,
                                                 const u16* __restrict__ wmod, const u16* __restrict__ wqkv,
                                                 char* __restrict__ ws) {
  __shared__ float red[64 * 33];
  const int tid = threadIdx.x;
  TDECL; TS(0);
  if (blockIdx.x >= 148) {
    u32x4 acc_ = {0u, 0u, 0u, 0u};
    const int b = blockIdx.x - 148;
    if (b < 8) {
      TOUCH_CODE("k1_qkv", 9088)
      TOUCH_CODE("k2_attn", 4096)
      TOUCH_CODE("k3_wo", 2304)
      TOUCH_CODE("k4_gateup", 7296)
      TOUCH_CODE("k5_down", 1536)
    } else {
#pragma unroll
      for (int i = 0; i < 3; ++i) {
        const int ch = b - 8 + 100 * i;
        if (ch < 294) acc_ ^= *(const u32x4*)((const char*)wqkv + (size_t)ch * 4096 + tid * 16);
      }
    }
    asm volatile("" ::"v"(acc_));
    return;
  }
  if (blockIdx.x < 84) {
    const int n0 = blockIdx.x * 32;
    const int cq = tid & 3, rg = tid >> 2;
    u32x4 w[7];
    float cv[7];
#pragma unroll
    for (int i = 0; i < 7; ++i) {
      w[i] = *(const u32x4*)(wmod + (size_t)(rg + 64 * i) * 2688 + n0 + cq * 8);
      cv[i] = bf2f(c[rg + 64 * i]);
    }
    float acc[8];
#pragma unroll
    for (int j = 0; j < 8; ++j) acc[j] = 0.f;
#pragma unroll
    for (int i = 0; i < 7; ++i) {
      const float s = bf_round(cv[i] / (1.f + __expf(-cv[i])));
#pragma unroll
      for (int j = 0; j < 4; ++j) {
        acc[2 * j] += s * bf_lo(w[i][j]);
        acc[2 * j + 1] += s * bf_hi(w[i][j]);
      }
    }
#pragma unroll
    for (int j = 0; j < 8; ++j) red[rg * 33 + cq * 8 + j] = acc[j];
    TS(1);
    __syncthreads();
    const int col = tid >> 3, part = tid & 7;
    float s = 0.f;
#pragma unroll
    for (int i = 0; i < 8; ++i) s += red[(part * 8 + i) * 33 + col];
    s += __shfl_xor(s, 1);
    s += __shfl_xor(s, 2);
    s += __shfl_xor(s, 4);
    if (part == 0) {
      const int n = n0 + col;
      float v = bf_round(s);
      const int sec = n / 448;
      if (sec == 1 || sec == 4) v = bf_round(1.f + v);
      ((float*)(ws + WS_MOD))[n] = v;
    }
  } else {
    const int lane = tid & 63, wid = tid >> 6;
    const int row = (blockIdx.x - 84) * 32 + wid * 8 + (lane >> 3);
    const int i8 = lane & 7;
    u32x4 v[7];
#pragma unroll
    for (int m = 0; m < 7; ++m) v[m] = *(const u32x4*)(x + (size_t)row * 448 + (i8 + 8 * m) * 8);
    float s = 0.f;
#pragma unroll
    for (int m = 0; m < 7; ++m)
#pragma unroll
      for (int j = 0; j < 4; ++j) s += bf_lo(v[m][j]) + bf_hi(v[m][j]);
    s += __shfl_xor(s, 1);
    s += __shfl_xor(s, 2);
    s += __shfl_xor(s, 4);
    const float mean = s * (1.f / 448.f);
    TS(1);
    float q = 0.f;
#pragma unroll
    for (int m = 0; m < 7; ++m)
#pragma unroll
      for (int j = 0; j < 4; ++j) {
        const float a = bf_lo(v[m][j]) - mean, b = bf_hi(v[m][j]) - mean;
        q += a * a + b * b;
      }
    q += __shfl_xor(q, 1);
    q += __shfl_xor(q, 2);
    q += __shfl_xor(q, 4);
    const float rstd = rsqrtf(q * (1.f / 448.f) + 1e-5f);
    char* dst = ws + WS_XN + (size_t)(row >> 4) * 14 * 1024 + (row & 15) * 16;
#pragma unroll
    for (int m = 0; m < 7; ++m) {
      u32x4 o;
#pragma unroll
      for (int j = 0; j < 4; ++j) o[j] = pk_f16((bf_lo(v[m][j]) - mean) * rstd, (bf_hi(v[m][j]) - mean) * rstd);
      const int ch = i8 + 8 * m;
      st128(dst + (ch >> 2) * 1024 + (ch & 3) * 256, o);
    }
  }
  TS(41); TFLUSH(0);
}

template <int OFF> DI void glds_o(const void* sbase, unsigned voff, unsigned laddr) {
  asm volatile("s_add_u32 m0, %2, %3\n\ts_nop 0\n\tglobal_load_lds_dwordx4 %0, %1" ::"v"(voff), "s"(sbase), "s"(laddr), "i"(OFF) : "scc");
}
template <int OFF> DI void glds_o48(const void* sbase, unsigned voff, unsigned laddr) {
  asm volatile("s_add_u32 m0, %2, %3\n\ts_mov_b32 exec_hi, 0xffff\n\tglobal_load_lds_dwordx4 %0, %1\n\ts_mov_b32 exec_hi, -1"
               ::"v"(voff), "s"(sbase), "s"(laddr), "i"(OFF) : "scc");
}

template <int RB, int NSEG, int NBS, int KS, int SK, int D, int LDW, bool PERM, bool SWAP>
struct Gemm {
  static constexpr int NST = KS / SK;
  static constexpr int NSLOT = D + 3;
  static constexpr int RS = 32 * NBS;
  static constexpr int PPK = 4 * RB + NSEG * NBS;
  static constexpr int STG = SK * PPK * 1024;
  static constexpr int LDS_BYTES = NSLOT * STG;
  static constexpr int NBI = NBS == 7 ? 2 : 1;
  static constexpr int CNT = SK * (RB + NSEG * NBI);
  static_assert(D + 1 < NST && D * CNT < 64 && (SK == 1 || SK == 2) && KS % 2 == 0, "cfg");

  template <class HK>
  static DI void load(int wl, unsigned lds0, const char* Ablk, const u16* W0, const u16* W1, int n0, int lane, HK&& hk TARG) {
    unsigned va[SK][RB], vb[SK][NBI];
#pragma unroll
    for (int i = 0; i < NBI; ++i) {
      const int e = wl * (NBS * 16) + i * 64 + lane;
      int pos, col;
      if constexpr (NBS == 7) {
        pos = (e * 4682) >> 16;
        col = (e - pos * 14) * 8;
        pos &= 31;
      } else {
        pos = e >> 3;
        const int slot = e & 7;
        col = 16 * ((slot >> 1) ^ ((pos >> 1) & 3)) + 8 * (slot & 1);
      }
      const int kk = PERM ? (8 * ((pos >> 2) & 3) + 4 * (pos >> 4) + (pos & 3)) : pos;
#pragma unroll
      for (int k = 0; k < SK; ++k) vb[k][i] = LDW ? ((k * 32 + kk) * LDW + n0 + col) * 2 : k * NBS * 1024 + e * 16;
    }
#pragma unroll
    for (int k = 0; k < SK; ++k)
#pragma unroll
      for (int r = 0; r < RB; ++r) va[k][r] = ((wl * RB + r) * KS + k) * 1024 + lane * 16;
    const unsigned mA = lds0 + wl * RB * 1024, mB = lds0 + 4 * RB * 1024 + wl * NBS * 256;
    const char* pa = Ablk;
    const char* p0 = (const char*)W0;
    const char* p1 = (const char*)W1;
    unsigned so = 0;
    auto issue = [&]() {
      const unsigned a = mA + so, b = mB + so;
      sfor<0, SK>([&](auto kk) {
        constexpr int k = decltype(kk)::v;
        sfor<0, RB>([&](auto rr) { glds_o<(k * PPK + decltype(rr)::v) * 1024>(pa, va[k][decltype(rr)::v], a); });
        glds_o<k * PPK * 1024>(p0, vb[k][0], b);
        if constexpr (NBI == 2) glds_o48<k * PPK * 1024 + 1024>(p0, vb[k][1], b);
        if constexpr (NSEG == 2) {
          glds_o<(k * PPK + NBS) * 1024>(p1, vb[k][0], b);
          if constexpr (NBI == 2) glds_o48<(k * PPK + NBS) * 1024 + 1024>(p1, vb[k][1], b);
        }
      });
      pa += SK * 1024;
      p0 += LDW ? SK * 32 * LDW * 2 : SK * NBS * 1024;
      p1 += LDW ? SK * 32 * LDW * 2 : SK * NBS * 1024;
      so += STG;
      so = so == NSLOT * STG ? 0 : so;
    };
#pragma unroll 1
    for (int T = -(D + 1); T < NST - D - 1; ++T) {
      if (T >= 0) wait_vm_bar<D * CNT>();
      issue();
      hk(T);
    }
    TS(1);
    sfor<0, D + 1>([&](auto tt) { wait_vm_bar<(D - decltype(tt)::v) * CNT>(); });
    bar();
  }

  template <int NW, int WR, class XR, class XF>
  static DI void compute(const lds_char* lds, f32x4 (&acc)[WR][NSEG * NW], int wid, int ns, int lane, XR&& xr, XF&& xf TARG) {
    constexpr int NBP = NBS == 7 ? 1 : NW;
    constexpr int NG = NSEG * NW;
    const unsigned ap0 = lds_addr(lds) + wid * WR * 1024 + lane * 16;
    unsigned bp0[NBP];
    if constexpr (NBS == 7) {
      bp0[0] = lds_addr(lds) + 4 * RB * 1024 + (lane >> 2) * 224 + 8 * (lane & 3) + ns * 32;
    } else {
      const unsigned tb = (lane >> 2) * 128 + (((lane >> 3) & 3) << 5) + 8 * (lane & 3);
#pragma unroll
      for (int g = 0; g < NW; ++g) bp0[g] = lds_addr(lds) + 4 * RB * 1024 + (tb ^ ((ns + g) << 5));
    }
    unsigned ap = ap0, bp[NBP];
#pragma unroll
    for (int g = 0; g < NBP; ++g) bp[g] = bp0[g];
    u32x4 a[2][WR];
    u32x4 b[SK][NG];
    auto rdA = [&](auto hh, auto kk) {
#pragma unroll
      for (int rb = 0; rb < WR; ++rb)
        a[decltype(hh)::v][rb] = lds_rd128((const lds_char*)(uintptr_t)ap + (decltype(kk)::v * PPK + rb) * 1024);
    };
    auto rdB = [&](auto kk, auto gg) {
      constexpr int k = decltype(kk)::v, g = decltype(gg)::v;
      constexpr int off = (k * PPK + (g / NW) * NBS) * 1024;
      const lds_char* p;
      if constexpr (NBS == 7) p = (const lds_char*)(uintptr_t)bp[0] + off + (g % NW) * 32;
      else p = (const lds_char*)(uintptr_t)bp[g % NW] + off;
      const u32x2 b0 = lds_tr(p), b1 = lds_tr(p + 16 * RS);
      b[k][g] = u32x4{b0[0], b0[1], b1[0], b1[1]};
    };
#pragma unroll
    for (int rb = 0; rb < WR; ++rb)
#pragma unroll
      for (int g = 0; g < NG; ++g) asm volatile("" : "+v"(acc[rb][g]));
    TS(1);
    bar();
    TS(2); TC(50);
    sfor<0, SK>([&](auto kk) {
      rdA(kk, kk);
      if constexpr (SK == 1) xr(IC<2>{});
      sfor<0, NG>([&](auto gg) { rdB(kk, gg); });
    });
    __builtin_amdgcn_s_waitcnt(0xc07f);
    sfor<0, NG * WR>([&](auto gg) { xf(gg, IC<1>{}, a[0]); });
    unsigned so = 0;
#pragma unroll 1
    for (int it = 0; it < KS / 2; ++it) {
      sfor<0, 2>([&](auto hh) {
        constexpr int h = decltype(hh)::v;
        constexpr int kb = SK == 1 ? 0 : h;
        if constexpr (SK == 1 || h == 0) {
          so += STG;
          so = so == NSLOT * STG ? 0 : so;
          ap = ap0 + so;
#pragma unroll
          for (int g = 0; g < NBP; ++g) bp[g] = bp0[g] + so;
          bar();
        }
        if constexpr (SK == 1) {
          rdA(IC<h ^ 1>{}, IC<0>{});
          xr(hh);
        }
        sched_fence();
        sfor<0, NG>([&](auto gg) {
          constexpr int g = decltype(gg)::v;
          sfor<0, WR>([&](auto rr) {
            constexpr int rb = decltype(rr)::v;
            acc[rb][g] = SWAP ? mfma16(b[kb][g], a[h][rb], acc[rb][g]) : mfma16(a[h][rb], b[kb][g], acc[rb][g]);
            if constexpr (rb < WR - 1) {
              xf(IC<g * WR + rb>{}, hh, a[h ^ 1]);
              sched_fence();
            }
          });
          rdB(IC<kb>{}, gg);
          if constexpr (SK == 2 && g == NG - 1) rdA(hh, hh);
          xf(IC<g * WR + WR - 1>{}, hh, a[h ^ 1]);
          sched_fence();
        });
      });
    }
  }
};

struct NoX {
  template <class... A> DI void operator()(A&&...) const {}
};

constexpr int D1 = 4;
#define XF0 6
typedef Gemm<2, 1, 7, 14, 1, D1, 1344, true, true> G1S;

template <bool SWAP, int NW>
DI void k1_compute(const lds_char* lds, char* __restrict__ ws, int mt, int nt, int wid, int lane TARG) {
  constexpr int ns = 0;
  typedef Gemm<2, 1, 7, 14, 1, D1, 1344, true, SWAP> G;
  const lds_char* vec = lds + G::LDS_BYTES;
  f32x4 acc[2][NW];
#pragma unroll
  for (int rb = 0; rb < 2; ++rb)
#pragma unroll
    for (int nb = 0; nb < NW; ++nb) acc[rb][nb] = f32x4{0.f, 0.f, 0.f, 0.f};
  const int gq = lane >> 4, t = lane & 15;
  const lds_char* vp = vec + gq * 32;
  f32x4 sv[2], mv[2];
  G::template compute<NW, 2>(
      lds, acc, wid, ns, lane,
      [&](auto hh) {
        if constexpr (decltype(hh)::v == 2) {
          sv[0] = lds_rd128f(vp);
          sv[1] = lds_rd128f(vp + 16);
          mv[0] = lds_rd128f(vp + 1792);
          mv[1] = lds_rd128f(vp + 1792 + 16);
          vp += 128;
        }
      },
      [&](auto gg, auto, u32x4(&a)[2]) {
        constexpr int g = decltype(gg)::v - XF0;
        if constexpr (g >= 0 && g < 8) {
          constexpr int j = g >> 1, rb = g & 1;
          a[rb][j] = pk_bf16(mix_lo(a[rb][j], mv[j >> 1][(j & 1) * 2], sv[j >> 1][(j & 1) * 2]),
                             mix_hi(a[rb][j], mv[j >> 1][(j & 1) * 2 + 1], sv[j >> 1][(j & 1) * 2 + 1]));
          if constexpr ((g & 3) == 3) {
            sv[g >> 2] = lds_rd128f(vp + (g >> 2) * 16);
            mv[g >> 2] = lds_rd128f(vp + 1792 + (g >> 2) * 16);
            if constexpr (g == 7) vp += 128;
          }
        }
      } TPASS);
  TS(40); TC(51);
  const int sec = nt >> 2, h0 = (nt & 3) * 4;
  if constexpr (SWAP) {
    const float sc = sec == 0 ? 0.18898223650461362f * 1.4426950408889634f : 1.f;
    char* base = ws + (sec == 0 ? WS_Q : WS_K);
#pragma unroll
    for (int rb = 0; rb < 2; ++rb) {
      const int tb = mt * 8 + wid * 2 + rb;
#pragma unroll
      for (int nb = 0; nb < NW; ++nb) {
        const int Gi = 4 * (ns + nb) + gq;
        const int hd = (Gi * 37) >> 8, dg = Gi - 7 * hd;
        char* dst = base + ((size_t)(h0 + hd) * 128 + tb) * 1024 + ((dg >> 1) * 16 + t) * 16 + (dg & 1) * 8;
        const f32x4 v = acc[rb][nb];
        const unsigned lo = pk_bf16(v[0] * sc, v[1] * sc), hi = pk_bf16(v[2] * sc, v[3] * sc);
        if (dg == 6) st128(dst, u32x4{lo, hi, 0u, 0u});
        else st64(dst, u32x2{lo, hi});
      }
    }
  } else {
    const int s32 = mt * 4 + wid;
    char* base = ws + WS_V;
#pragma unroll
    for (int nb = 0; nb < NW; ++nb) {
      const int n = 16 * (ns + nb) + t;
      const int hd = (n * 147) >> 12, d = n - 28 * hd;
      char* dst = base + (((size_t)(h0 + hd) * 64 + s32) * 2 + (d >> 4)) * 1024 + (gq * 16 + (d & 15)) * 16;
      const f32x4 v0 = acc[0][nb], v1 = acc[1][nb];
      st128(dst, u32x4{pk_bf16(v0[0], v0[1]), pk_bf16(v0[2], v0[3]), pk_bf16(v1[0], v1[1]), pk_bf16(v1[2], v1[3])});
    }
    if (lane < 16) {
      char* dst = base + (((size_t)(h0 + (lane >> 2)) * 64 + s32) * 2 + 1) * 1024 + ((lane & 3) * 16 + 12) * 16;
      st128(dst, u32x4{0x3f803f80u, 0x3f803f80u, 0x3f803f80u, 0x3f803f80u});
    }
  }
}

extern "C" __global__ __launch_bounds__(512, 1) void k1_qkv(const u16* __restrict__ wqkv, const u16* __restrict__ wg,
                                                 const u16* __restrict__ wu, const u16* __restrict__ wo,
    const u16* __restrict__ wd, char* __restrict__ ws) {
  TDECL; TS(0);
  extern __shared__ __attribute__((aligned(16))) char smem[];
  lds_char* lds = (lds_char*)smem;
  const int lane = threadIdx.x & 63, wid = __builtin_amdgcn_readfirstlane(threadIdx.x >> 6);
  const int bid = blockIdx.x, xcd = bid & 7, j = bid >> 3;
  if (j >= 24) {
    const int e = (j - 24) * 8 + xcd;
    const int mat = e >> 5, c0 = (e & 31) * 56;
    const u16* W = mat ? wu : wg;
    const int tid = threadIdx.x;
    float* red = (float*)smem;
    if (tid < 448) {
      const int rg = tid / 7, cq = tid - rg * 7;
      u32x4 w[7];
      float m[7], sb[7];
#pragma unroll
      for (int i = 0; i < 7; ++i) {
        const int k = rg + 64 * i;
        w[i] = *(const u32x4*)(W + (size_t)k * 1792 + c0 + cq * 8);
        sb[i] = ((const float*)(ws + WS_MOD))[3 * 448 + k];
        m[i] = ((const float*)(ws + WS_MOD))[4 * 448 + k];
      }
      float a1[8], a2[8];
#pragma unroll
      for (int q = 0; q < 8; ++q) a1[q] = a2[q] = 0.f;
#pragma unroll
      for (int i = 0; i < 7; ++i)
#pragma unroll
        for (int q = 0; q < 4; ++q) {
          a1[2 * q] += m[i] * bf_lo(w[i][q]);
          a1[2 * q + 1] += m[i] * bf_hi(w[i][q]);
          a2[2 * q] += sb[i] * bf_lo(w[i][q]);
          a2[2 * q + 1] += sb[i] * bf_hi(w[i][q]);
        }
#pragma unroll
      for (int q = 0; q < 8; ++q) {
        red[rg * 113 + cq * 8 + q] = a1[q];
        red[rg * 113 + 56 + cq * 8 + q] = a2[q];
      }
    }
    __syncthreads();
    if (tid < 448) {
      const int col = tid >> 2, part = tid & 3;
      float s = 0.f;
#pragma unroll
      for (int i = 0; i < 16; ++i) s += red[(part * 16 + i) * 113 + col];
      s += __shfl_xor(s, 1);
      s += __shfl_xor(s, 2);
      if (part == 0) {
        const int which = col >= 56, n = c0 + col - which * 56;
        const int nt = n / 112;
        ((float*)(ws + WS_U))[(nt * 4 + mat * 2 + which) * 112 + n - nt * 112] = s;
      }
    }
    u32x4 tw = {0u, 0u, 0u, 0u};
#pragma unroll
    for (int i = 0; i < 4; ++i) {
      const int ch = e + 64 * i;
      if (ch < 245) tw ^= *(const u32x4*)((ch < 49 ? (const char*)wo + (size_t)ch * 8192 : (const char*)wd + (size_t)(ch - 49) * 8192) + tid * 16);
    }
    asm volatile("" ::"v"(tw));
    TS(41); TFLUSH(1);
    return;
  }
  const int mt = (xcd >> 1) * 4 + (j & 3), nt = (xcd & 1) * 6 + (j >> 2);
  if (wid < 4) {
    const int off = wid * 1024 + lane * 16;
    glds(ws + WS_MOD, off < 3584 ? off : 0, lds_addr(lds) + G1S::LDS_BYTES + wid * 1024);
    G1S::load(wid, lds_addr(lds), ws + WS_XN + (size_t)mt * 8 * 14 * 1024, wqkv, wqkv, nt * 112, lane, NoX{} TPASS);
  } else {
    if (nt < 8) k1_compute<true, 7>(lds, ws, mt, nt, wid - 4, lane TPASS);
    else k1_compute<false, 7>(lds, ws, mt, nt, wid - 4, lane TPASS);
  }
  TS(41); TFLUSH(1);
}

DI void k2_wave(const lds_char* lds, char* __restrict__ ws, int qt, int head, int wid, int lane) {
  const int gq = lane >> 4, t = lane & 15;
  const int lp0 = wid >> 1;
  const int qb = 8 * qt + wid;
  const lds_char* kp = lds + lp0 * 2048 + lane * 16;
  const lds_char* vp = lds + 24576 + lp0 * 2048 + lane * 16;
  const u32x4 qf = lds_rd128(lds + 49152 + wid * 1024 + lane * 16);
  f32x4 sc[18];
  const float NEG = -__builtin_inff();
#pragma unroll
  for (int i = 0; i < 18; ++i) {
    const u32x4 kf = lds_rd128(kp + i * 1024);
    sc[i] = mfma16(kf, qf, f32x4{0.f, 0.f, 0.f, 0.f});
  }
  const int d0 = 256 + 16 * (wid & 1) + t - 4 * gq;
#pragma unroll
  for (int r = 0; r < 4; ++r) {
    if (d0 - r >= 256) sc[0][r] = NEG;
    if (d0 - 16 - r >= 256) sc[1][r] = NEG;
    if (d0 - 256 - r < 0) sc[16][r] = NEG;
    if (d0 - 272 - r < 0) sc[17][r] = NEG;
  }
  if (qt < 2) {
    const int kb0 = 8 * qt - 16 + 2 * lp0;
#pragma unroll
    for (int i = 0; i < 16; ++i)
      if (kb0 + i < 0) sc[i] = f32x4{NEG, NEG, NEG, NEG};
  }
  float m = sc[16][0];
#pragma unroll
  for (int i = 0; i < 18; ++i) m = fmaxf(fmaxf(m, fmaxf(sc[i][0], sc[i][1])), fmaxf(sc[i][2], sc[i][3]));
  m = fmaxf(m, __shfl_xor(m, 16));
  m = fmaxf(m, __shfl_xor(m, 32));
  unsigned pp[18][2];
#pragma unroll
  for (int i = 0; i < 18; ++i) {
    pp[i][0] = pk_bf16(__builtin_amdgcn_exp2f(sc[i][0] - m), __builtin_amdgcn_exp2f(sc[i][1] - m));
    pp[i][1] = pk_bf16(__builtin_amdgcn_exp2f(sc[i][2] - m), __builtin_amdgcn_exp2f(sc[i][3] - m));
  }
  wait_vm_bar<0>();
  f32x4 o0 = {0.f, 0.f, 0.f, 0.f}, o1 = {0.f, 0.f, 0.f, 0.f};
#pragma unroll
  for (int p = 0; p < 9; ++p) {
    const u32x4 pf = {pp[2 * p][0], pp[2 * p][1], pp[2 * p + 1][0], pp[2 * p + 1][1]};
    const u32x4 v0 = lds_rd128(vp + p * 2048), v1 = lds_rd128(vp + p * 2048 + 1024);
    o0 = mfma16(v0, pf, o0);
    o1 = mfma16(v1, pf, o1);
  }
  const float sum = __shfl(o1[0], 48 + t);
  const float inv = __builtin_amdgcn_rcpf(sum);
  char* base = ws + WS_O + (size_t)qb * 14 * 1024;
  {
    const int Gi = 7 * head + gq;
    st64(base + (Gi >> 3) * 1024 + (((Gi >> 1) & 3) * 16 + t) * 16 + (Gi & 1) * 8, u32x2{pk_bf16(o0[0] * inv, o0[1] * inv), pk_bf16(o0[2] * inv, o0[3] * inv)});
  }
  if (gq < 3) {
    const int Gi = 7 * head + 4 + gq;
    st64(base + (Gi >> 3) * 1024 + (((Gi >> 1) & 3) * 16 + t) * 16 + (Gi & 1) * 8, u32x2{pk_bf16(o1[0] * inv, o1[1] * inv), pk_bf16(o1[2] * inv, o1[3] * inv)});
  }
}

extern "C" __global__ __launch_bounds__(512, 2) void k2_attn(char* __restrict__ ws) {
  TDECL; TS(0);
  extern __shared__ __attribute__((aligned(16))) char smem[];
  lds_char* lds = (lds_char*)smem;
  const int lane = threadIdx.x & 63, wid = __builtin_amdgcn_readfirstlane(threadIdx.x >> 6);
  const int bid = blockIdx.x, xcd = bid & 7, j = bid >> 3;
  const int head = xcd * 2 + (j & 1), qt = j >> 1;
  const unsigned l0 = lds_addr(lds);
  glds(ws + WS_Q + ((size_t)head * 128 + 8 * qt + wid) * 1024, lane * 16, l0 + 49152 + wid * 1024);
#pragma unroll
  for (int i = 0; i < 3; ++i) {
    const int lk = wid * 3 + i;
    int tb = 8 * qt - 16 + lk;
    tb = tb < 0 ? 0 : tb;
    glds(ws + WS_K + ((size_t)head * 128 + tb) * 1024, lane * 16, l0 + lk * 1024);
  }
#pragma unroll
  for (int i = 0; i < 3; ++i) {
    const int lv = wid * 3 + i;
    int f = (4 * qt - 8) * 2 + lv;
    f = f < 0 ? (lv & 1) : f;
    glds(ws + WS_V + ((size_t)head * 128 + f) * 1024, lane * 16, l0 + 24576 + lv * 1024);
  }
  TS(1);
  wait_vm_bar<3>();
  TS(2);
  k2_wave(lds, ws, qt, head, wid, lane);
  TS(41); TFLUSH(2);
}

typedef Gemm<1, 1, 4, 14, 2, 4, 448, true, true> G3;
extern "C" __global__ __launch_bounds__(512, 1) void k3_wo(const u16* __restrict__ x, const u16* __restrict__ wo, char* __restrict__ ws) {
  TDECL; TS(0);
  extern __shared__ __attribute__((aligned(16))) char smem[];
  lds_char* lds = (lds_char*)smem;
  const int lane = threadIdx.x & 63, wid = __builtin_amdgcn_readfirstlane(threadIdx.x >> 6);
  const int bid = blockIdx.x, xcd = bid & 7, j = bid >> 3;
  const int mt = xcd * 4 + (j & 3), nt = j >> 2;
  if (wid >= 4) {
    G3::load(wid - 4, lds_addr(lds), ws + WS_O + (size_t)mt * 4 * 14 * 1024, wo, wo, nt * 64, lane, NoX{} TPASS);
    TS(41); TFLUSH(3);
    return;
  }
  const int gq = lane >> 4, t = lane & 15;
  const int tb = mt * 4 + wid, row = tb * 16 + t;
  u32x2 xr[4];
  f32x4 ga[4], m2[4];
#pragma unroll
  for (int nb = 0; nb < 4; ++nb) {
    xr[nb] = *(const u32x2*)(x + (size_t)row * 448 + nt * 64 + nb * 16 + gq * 4);
    ga[nb] = *(const f32x4*)((const float*)(ws + WS_MOD) + 2 * 448 + nt * 64 + nb * 16 + gq * 4);
    m2[nb] = *(const f32x4*)((const float*)(ws + WS_MOD) + 4 * 448 + nt * 64 + nb * 16 + gq * 4);
  }
  f32x4 acc[1][4];
#pragma unroll
  for (int nb = 0; nb < 4; ++nb) acc[0][nb] = f32x4{0.f, 0.f, 0.f, 0.f};
  G3::compute<4, 1>(lds, acc, wid, 0, lane, NoX{}, NoX{} TPASS);
  TS(40); TC(51);
  unsigned o[4][2], om[4][2];
  float v[4][4];
  float s = 0.f;
#pragma unroll
  for (int nb = 0; nb < 4; ++nb) {
    o[nb][0] = pk_bf16(bf_lo(xr[nb][0]) + ga[nb][0] * acc[0][nb][0], bf_hi(xr[nb][0]) + ga[nb][1] * acc[0][nb][1]);
    o[nb][1] = pk_bf16(bf_lo(xr[nb][1]) + ga[nb][2] * acc[0][nb][2], bf_hi(xr[nb][1]) + ga[nb][3] * acc[0][nb][3]);
    v[nb][0] = bf_lo(o[nb][0]); v[nb][1] = bf_hi(o[nb][0]); v[nb][2] = bf_lo(o[nb][1]); v[nb][3] = bf_hi(o[nb][1]);
    om[nb][0] = pk_bf16(v[nb][0] * m2[nb][0], v[nb][1] * m2[nb][1]);
    om[nb][1] = pk_bf16(v[nb][2] * m2[nb][2], v[nb][3] * m2[nb][3]);
    s += (v[nb][0] + v[nb][1]) + (v[nb][2] + v[nb][3]);
  }
  char* dst = ws + WS_X1 + ((size_t)tb * 14 + nt * 2) * 1024 + lane * 16;
  st128(dst + (WS_XM - WS_X1), u32x4{om[0][0], om[0][1], om[1][0], om[1][1]});
  st128(dst + (WS_XM - WS_X1) + 1024, u32x4{om[2][0], om[2][1], om[3][0], om[3][1]});
  st128(dst, u32x4{o[0][0], o[0][1], o[1][0], o[1][1]});
  st128(dst + 1024, u32x4{o[2][0], o[2][1], o[3][0], o[3][1]});
  s += __shfl_xor(s, 16);
  s += __shfl_xor(s, 32);
  const float mc = s * (1.f / 64.f);
  float q = 0.f;
#pragma unroll
  for (int nb = 0; nb < 4; ++nb)
#pragma unroll
    for (int r = 0; r < 4; ++r) q += (v[nb][r] - mc) * (v[nb][r] - mc);
  q += __shfl_xor(q, 16);
  q += __shfl_xor(q, 32);
  if (gq == 0) *(f32x2*)(ws + WS_ST + ((size_t)row * 7 + nt) * 8) = f32x2{mc, q};
  TS(41); TFLUSH(3);
}

typedef Gemm<2, 2, 7, 14, 1, 3, 1792, false, true> G4;
template <int NW>
DI void k4_compute(const lds_char* lds, char* __restrict__ ws, int mt, int nt, int wid, int lane TARG) {
  const int odd = nt & 1;
  const int ns = NW == 4 ? 3 * odd : 4 - 4 * odd;
  const int gq = lane >> 4, t = lane & 15;
  f32x4 acc[2][2 * NW];
#pragma unroll
  for (int rb = 0; rb < 2; ++rb)
#pragma unroll
    for (int nb = 0; nb < 2 * NW; ++nb) acc[rb][nb] = f32x4{0.f, 0.f, 0.f, 0.f};
  G4::compute<NW, 2>(lds, acc, wid, ns, lane, NoX{}, NoX{} TPASS);
  TS(40); TC(51);
  const lds_char* ex = lds + G4::LDS_BYTES;
  const lds_char* up = ex + gq * 16 + ns * 64;
  const int B0 = nt * 7 + ns;
  typedef const __attribute__((address_space(3))) f32x2* lp2;
  const f32x2 st0 = *(lp2)(ex + 9216 + (wid * 32 + t) * 8), st1 = *(lp2)(ex + 9216 + (wid * 32 + 16 + t) * 8);
  char* base = ws + WS_HH + (size_t)(mt * 8 + wid * 2) * 56 * 1024 + lane * 16 + (B0 >> 1) * 1024;
  unsigned o[2][NW][2];
#pragma unroll
  for (int nb = 0; nb < NW; ++nb) {
    const f32x4 u1g = lds_rd128f(up + nb * 64), u2g = lds_rd128f(up + nb * 64 + 448);
    const f32x4 u1u = lds_rd128f(up + nb * 64 + 896), u2u = lds_rd128f(up + nb * 64 + 1344);
#pragma unroll
    for (int rb = 0; rb < 2; ++rb) {
      const f32x2 st = rb ? st1 : st0;
      const f32x2 nm = {st[0], st[0]}, rs2 = {st[1], st[1]};
#pragma unroll
      for (int r = 0; r < 2; ++r) {
        const f32x2 ag = {acc[rb][nb][2 * r], acc[rb][nb][2 * r + 1]}, au = {acc[rb][NW + nb][2 * r], acc[rb][NW + nb][2 * r + 1]};
        const f32x2 gv = rs2 * ag + (nm * f32x2{u1g[2 * r], u1g[2 * r + 1]} + f32x2{u2g[2 * r], u2g[2 * r + 1]});
        const f32x2 uv = rs2 * au + (nm * f32x2{u1u[2 * r], u1u[2 * r + 1]} + f32x2{u2u[2 * r], u2u[2 * r + 1]});
        const f32x2 z = gv * (gv * gv * f32x2{-0.1029432397f, -0.1029432397f} + f32x2{-2.302208198f, -2.302208198f});
        const f32x2 d = f32x2{__builtin_amdgcn_exp2f(z[0]), __builtin_amdgcn_exp2f(z[1])} + f32x2{1.f, 1.f};
        const f32x2 h = gv * uv * f32x2{__builtin_amdgcn_rcpf(d[0]), __builtin_amdgcn_rcpf(d[1])};
        o[rb][nb][r] = pk_bf16(h[0], h[1]);
      }
      if (NW == 4 && (nb & 1))
        st128(base + rb * 56 * 1024 + (nb >> 1) * 1024, u32x4{o[rb][nb - 1][0], o[rb][nb - 1][1], o[rb][nb][0], o[rb][nb][1]});
    }
  }
  if constexpr (NW == 3) {
#pragma unroll
    for (int rb = 0; rb < 2; ++rb) {
      char* bs = base + rb * 56 * 1024;
      if (odd) {
        st64(bs + 8, u32x2{o[rb][0][0], o[rb][0][1]});
        st128(bs + 1024, u32x4{o[rb][1][0], o[rb][1][1], o[rb][2][0], o[rb][2][1]});
      } else {
        st128(bs, u32x4{o[rb][0][0], o[rb][0][1], o[rb][1][0], o[rb][1][1]});
        st64(bs + 1024, u32x2{o[rb][2][0], o[rb][2][1]});
      }
    }
  }
}

extern "C" __global__ __launch_bounds__(768, 3) void k4_gateup(const u16* __restrict__ wg, const u16* __restrict__ wu, char* __restrict__ ws) {
  TDECL; TS(0);
  extern __shared__ __attribute__((aligned(16))) char smem[];
  lds_char* lds = (lds_char*)smem;
  const int lane = threadIdx.x & 63, wid = __builtin_amdgcn_readfirstlane(threadIdx.x >> 6);
  const int bid = blockIdx.x, xcd = bid & 7, j = bid >> 3;
  const int mt = (xcd >> 2) * 8 + (j & 7), nt = (xcd & 3) * 4 + (j >> 3);
  if (wid < 4) {
    const int wl = wid;
    const unsigned ex = lds_addr(lds) + G4::LDS_BYTES;
    if (wl < 2) {
      const int off = wl * 1024 + lane * 16;
      glds(ws + WS_U + (size_t)nt * 1792, off < 1792 ? off : 0, ex + wl * 1024);
    }
#pragma unroll
    for (int i = 0; i < 2; ++i) {
      const int o2 = (wl * 2 + i) * 1024 + lane * 16;
      glds(ws + WS_ST + (size_t)mt * 7168, o2 < 7168 ? o2 : 0, ex + 2048 + (wl * 2 + i) * 1024);
    }
    float mc[7], q = 0.f, mean = 0.f;
#pragma unroll
    for (int c = 0; c < 7; ++c) mc[c] = 0.f;
    const int row = wl * 32 + (lane & 31);
    G4::load(wl, lds_addr(lds), ws + WS_XM + (size_t)mt * 8 * 14 * 1024, wg, wu, nt * 112, lane, [&](int T) {
      if (T == 1) {
        const lds_char* sp = lds + G4::LDS_BYTES + 2048 + row * 56;
        float sm = 0.f;
#pragma unroll
        for (int c = 0; c < 7; ++c) {
          const f32x2 e = *(const __attribute__((address_space(3))) f32x2*)(sp + c * 8);
          mc[c] = e[0]; q += e[1]; sm += e[0];
        }
        mean = sm * (1.f / 7.f);
      }
      if (T == 3) {
#pragma unroll
        for (int c = 0; c < 7; ++c) q += 64.f * (mc[c] - mean) * (mc[c] - mean);
        const float rs = rsqrtf(q * (1.f / 448.f) + 1e-5f);
        *(__attribute__((address_space(3))) f32x2*)(lds + G4::LDS_BYTES + 9216 + row * 8) = f32x2{-mean * rs, rs};
        asm volatile("s_waitcnt lgkmcnt(0)" ::: "memory");
      }
    } TPASS);
  } else if (wid < 8) {
    k4_compute<4>(lds, ws, mt, nt, wid - 4, lane TPASS);
  } else {
    k4_compute<3>(lds, ws, mt, nt, wid - 8, lane TPASS);
  }
  TS(41); TFLUSH(4);
}

typedef Gemm<1, 1, 4, 56, 2, 4, 448, false, true> G5;
extern "C" __global__ __launch_bounds__(512, 1) void k5_down(const u16* __restrict__ wd, char* __restrict__ ws, u16* __restrict__ out) {
  TDECL; TS(0);
  extern __shared__ __attribute__((aligned(16))) char smem[];
  lds_char* lds = (lds_char*)smem;
  const int lane = threadIdx.x & 63, wid = __builtin_amdgcn_readfirstlane(threadIdx.x >> 6);
  const int bid = blockIdx.x, xcd = bid & 7, j = bid >> 3;
  const int mt = xcd * 4 + (j & 3), nt = j >> 2;
  if (wid >= 4) {
    G5::load(wid - 4, lds_addr(lds), ws + WS_HH + (size_t)mt * 4 * 56 * 1024, wd, wd, nt * 64, lane, NoX{} TPASS);
    TS(41); TFLUSH(5);
    return;
  }
  const int gq = lane >> 4, t = lane & 15, wr = wid >> 1, wc = wid & 1;
  const int tb = mt * 4 + wr * 2;
  u32x4 xr[2];
  f32x4 gb[2];
#pragma unroll
  for (int i = 0; i < 2; ++i) xr[i] = *(const u32x4*)(ws + WS_X1 + ((size_t)(tb + i) * 14 + nt * 2 + wc) * 1024 + lane * 16);
#pragma unroll
  for (int nb = 0; nb < 2; ++nb) gb[nb] = *(const f32x4*)((const float*)(ws + WS_MOD) + 5 * 448 + nt * 64 + wc * 32 + nb * 16 + gq * 4);
  f32x4 acc[2][2];
#pragma unroll
  for (int nb = 0; nb < 2; ++nb) acc[0][nb] = acc[1][nb] = f32x4{0.f, 0.f, 0.f, 0.f};
  G5::compute<2, 2>(lds, acc, wr, wc * 2, lane, NoX{}, NoX{} TPASS);
  TS(40); TC(51);
#pragma unroll
  for (int rb = 0; rb < 2; ++rb) {
    unsigned lo[2], hi[2];
#pragma unroll
    for (int nb = 0; nb < 2; ++nb) {
      const unsigned x0 = xr[rb][nb * 2], x1 = xr[rb][nb * 2 + 1];
      lo[nb] = pk_bf16(bf_lo(x0) + gb[nb][0] * acc[rb][nb][0], bf_hi(x0) + gb[nb][1] * acc[rb][nb][1]);
      hi[nb] = pk_bf16(bf_lo(x1) + gb[nb][2] * acc[rb][nb][2], bf_hi(x1) + gb[nb][3] * acc[rb][nb][3]);
    }
    const auto l = __builtin_amdgcn_permlane16_swap(lo[0], lo[1], false, false);
    const auto h = __builtin_amdgcn_permlane16_swap(hi[0], hi[1], false, false);
    u16* dst = out + (size_t)((tb + rb) * 16 + t) * 448 + nt * 64 + wc * 32 + (gq & 1) * 16 + (gq >> 1) * 8;
    st128(dst, u32x4{l[0], h[0], l[1], h[1]});
  }
  TS(41); TFLUSH(5);
}

extern "C" void kernel_launch(void* const* d_in, const int* in_sizes, int n_in, void* d_out, int out_size, void* d_ws,
                              size_t ws_size, hipStream_t stream) {
  const u16* x = (const u16*)d_in[0];
  const u16* wqkv = (const u16*)d_in[1];
  const u16* wo = (const u16*)d_in[2];
  const u16* wg = (const u16*)d_in[4];
  const u16* wu = (const u16*)d_in[5];
  const u16* wd = (const u16*)d_in[6];
  const u16* c = (const u16*)d_in[7];
  const u16* wmod = (const u16*)d_in[8];
  char* ws = (char*)d_ws;
  constexpr int L1 = G1S::LDS_BYTES + 4096;
  constexpr int L2 = 49152 + 8192;
  constexpr int L3 = G3::LDS_BYTES;
  constexpr int L4 = G4::LDS_BYTES + 2048 + 8192;
  constexpr int L5 = G5::LDS_BYTES;
  static_assert(L1 <= 163840 && L3 <= 163840 && L4 <= 163840 && L5 <= 163840, "lds");
  (void)hipFuncSetAttribute((const void*)k1_qkv, hipFuncAttributeMaxDynamicSharedMemorySize, L1);
  (void)hipFuncSetAttribute((const void*)k3_wo, hipFuncAttributeMaxDynamicSharedMemorySize, L3);
  (void)hipFuncSetAttribute((const void*)k4_gateup, hipFuncAttributeMaxDynamicSharedMemorySize, L4);
  (void)hipFuncSetAttribute((const void*)k5_down, hipFuncAttributeMaxDynamicSharedMemorySize, L5);
  k0_mod_ln<<<256, 256, 0, stream>>>(x, c, wmod, wqkv, ws);
  k1_qkv<<<256, 512, L1, stream>>>(wqkv, wg, wu, wo, wd, ws);
  k2_attn<<<256, 512, L2, stream>>>(ws);
  k3_wo<<<224, 512, L3, stream>>>(x, wo, ws);
  k4_gateup<<<256, 768, L4, stream>>>(wg, wu, ws);
  k5_down<<<224, 512, L5, stream>>>(wd, ws, (u16*)d_out);
}
```

```cpp
#include <hip/hip_runtime.h>
#include <hip/hip_bf16.h>
#include <stdint.h>

typedef __attribute__((ext_vector_type(8))) __bf16 bf16x8;
typedef __attribute__((ext_vector_type(2))) __bf16 bf16x2;
typedef __attribute__((ext_vector_type(2))) _Float16 f16x2;
typedef __attribute__((ext_vector_type(4))) float f32x4;
typedef __attribute__((ext_vector_type(2))) float f32x2;
typedef __attribute__((ext_vector_type(4))) short s16x4;
typedef __attribute__((ext_vector_type(4))) unsigned u32x4;
typedef __attribute__((ext_vector_type(2))) unsigned u32x2;
typedef __attribute__((address_space(3))) char lds_char;
typedef unsigned short u16;

#define DI __device__ __forceinline__
#define TDECL
#define TS(i)
#define TC(i)
#define TFLUSH(k)
#define TARG
#define TPASS

constexpr size_t WS_MOD = 0;
constexpr size_t WS_U = 16384;
constexpr size_t WS_XN = 65536;
constexpr size_t WS_Q = WS_XN + 128 * 14 * 1024;
constexpr size_t WS_K = WS_Q + 16 * 128 * 1024;
constexpr size_t WS_V = WS_K + 16 * 128 * 1024;
constexpr size_t WS_O = WS_V + 16 * 128 * 1024;
constexpr size_t WS_X1 = WS_O + 128 * 14 * 1024;
constexpr size_t WS_XM = WS_X1 + 128 * 14 * 1024;
constexpr size_t WS_ST = WS_XM + 128 * 14 * 1024;
constexpr size_t WS_HH = WS_ST + 2048 * 7 * 8;

template <int N> struct IC { static constexpr int v = N; };
template <int I, int N, class F> DI void sfor(F&& f) {
  if constexpr (I < N) { f(IC<I>{}); sfor<I + 1, N>(f); }
}
template <int N> DI void wait_vm_bar() { asm volatile("s_waitcnt vmcnt(%0)\n\ts_barrier" ::"i"(N) : "memory"); }
DI void bar() { asm volatile("s_barrier" ::: "memory"); }
DI void sched_fence() { __builtin_amdgcn_sched_barrier(0); }

DI float bf_lo(unsigned u) { return __uint_as_float(u << 16); }
DI float bf_hi(unsigned u) { return __uint_as_float(u & 0xffff0000u); }
DI unsigned pk_bf16(float lo, float hi) {
  return __builtin_bit_cast(unsigned, __builtin_convertvector(f32x2{lo, hi}, bf16x2));
}
DI unsigned pk_f16(float lo, float hi) {
  return __builtin_bit_cast(unsigned, __builtin_convertvector(f32x2{lo, hi}, f16x2));
}
DI float h_lo(unsigned u) { return (float)__builtin_bit_cast(f16x2, u)[0]; }
DI float h_hi(unsigned u) { return (float)__builtin_bit_cast(f16x2, u)[1]; }
DI float mix_lo(unsigned h, float m, float a) {
  float r;
  asm("v_fma_mix_f32 %0, %1, %2, %3 op_sel_hi:[1,0,0]" : "=v"(r) : "v"(h), "v"(m), "v"(a));
  return r;
}
DI float mix_hi(unsigned h, float m, float a) {
  float r;
  asm("v_fma_mix_f32 %0, %1, %2, %3 op_sel:[1,0,0] op_sel_hi:[1,0,0]" : "=v"(r) : "v"(h), "v"(m), "v"(a));
  return r;
}
DI float bf_round(float x) { return bf_lo(pk_bf16(x, 0.f)); }
DI float bf2f(u16 v) { return __uint_as_float(((unsigned)v) << 16); }

DI void glds(const void* sbase, unsigned voff, unsigned laddr) {
  asm volatile("s_mov_b32 m0, %2\n\ts_nop 0\n\tglobal_load_lds_dwordx4 %0, %1" ::"v"(voff), "s"(sbase), "s"(laddr));
}
DI void pf(unsigned& d, const void* p) {}
DI void pf_done(unsigned d) { asm volatile("s_waitcnt vmcnt(0)" ::"v"(d)); }
#define STP "sc1"
DI void st128(void* p, u32x4 v) { asm volatile("global_store_dwordx4 %0, %1, off " STP "\n\ts_nop 1" ::"v"(p), "v"(v) : "memory"); }
DI void st64(void* p, u32x2 v) { asm volatile("global_store_dwordx2 %0, %1, off " STP "\n\ts_nop 1" ::"v"(p), "v"(v) : "memory"); }
DI unsigned lds_addr(const lds_char* p) { return (unsigned)(uintptr_t)p; }
DI u32x4 lds_rd128(const lds_char* p) { return *(const __attribute__((address_space(3))) u32x4*)p; }
DI f32x4 lds_rd128f(const lds_char* p) { return *(const __attribute__((address_space(3))) f32x4*)p; }
DI u32x2 lds_tr(const lds_char* p) {
  return __builtin_bit_cast(u32x2, __builtin_amdgcn_ds_read_tr16_b64_v4i16((__attribute__((address_space(3))) s16x4*)p));
}
DI f32x4 mfma16(u32x4 a, u32x4 b, f32x4 c) {
  return __builtin_amdgcn_mfma_f32_16x16x32_bf16(__builtin_bit_cast(bf16x8, a), __builtin_bit_cast(bf16x8, b), c, 0, 0, 0);
}

__global__ __launch_bounds__(256) void k0_mod_ln(const u16* __restrict__ x, const u16* __restrict__ c,
                                                 const u16* __restrict__ wmod, const u16* __restrict__ wqkv,
                                                 const u16* wo, const u16* wg, const u16* wu, const u16* wd,
                                                 const u16* out, char* __restrict__ ws) {
  __shared__ float red[64 * 33];
  const int tid = threadIdx.x;
  TDECL; TS(0);
  if (blockIdx.x >= 148) {
    u32x4 acc_ = {0u, 0u, 0u, 0u};
    const int b = blockIdx.x - 148;
    if (b < 8) {
      const size_t o = (size_t)tid * 65536;
      acc_[0] = *(const unsigned*)(ws + o);
      if (tid < 67) acc_[1] = *(const unsigned*)(ws + o + 256 * 65536);
      const int q = tid >> 5, r = tid & 31;
      const u16* bp = q == 0 ? wqkv : q == 1 ? wo : q == 2 ? wg : q == 3 ? wu : q == 4 ? wd : out;
      const int nc = q == 0 ? 19 : q == 1 ? 7 : q < 5 ? 25 : q == 5 ? 28 : 0;
      if (r < nc) acc_[2] = *(const unsigned*)((const char*)bp + (size_t)r * 65536);
    } else {
#pragma unroll
      for (int i = 0; i < 3; ++i) {
        const int ch = b - 8 + 100 * i;
        if (ch < 294) acc_ ^= *(const u32x4*)((const char*)wqkv + (size_t)ch * 4096 + tid * 16);
      }
    }
    asm volatile("" ::"v"(acc_));
    return;
  }
  if (blockIdx.x < 84) {
    const int n0 = blockIdx.x * 32;
    const int cq = tid & 3, rg = tid >> 2;
    u32x4 w[7];
    float cv[7];
#pragma unroll
    for (int i = 0; i < 7; ++i) {
      w[i] = *(const u32x4*)(wmod + (size_t)(rg + 64 * i) * 2688 + n0 + cq * 8);
      cv[i] = bf2f(c[rg + 64 * i]);
    }
    float acc[8];
#pragma unroll
    for (int j = 0; j < 8; ++j) acc[j] = 0.f;
#pragma unroll
    for (int i = 0; i < 7; ++i) {
      const float s = bf_round(cv[i] / (1.f + __expf(-cv[i])));
#pragma unroll
      for (int j = 0; j < 4; ++j) {
        acc[2 * j] += s * bf_lo(w[i][j]);
        acc[2 * j + 1] += s * bf_hi(w[i][j]);
      }
    }
#pragma unroll
    for (int j = 0; j < 8; ++j) red[rg * 33 + cq * 8 + j] = acc[j];
    TS(1);
    __syncthreads();
    const int col = tid >> 3, part = tid & 7;
    float s = 0.f;
#pragma unroll
    for (int i = 0; i < 8; ++i) s += red[(part * 8 + i) * 33 + col];
    s += __shfl_xor(s, 1);
    s += __shfl_xor(s, 2);
    s += __shfl_xor(s, 4);
    if (part == 0) {
      const int n = n0 + col;
      float v = bf_round(s);
      const int sec = n / 448;
      if (sec == 1 || sec == 4) v = bf_round(1.f + v);
      ((float*)(ws + WS_MOD))[n] = v;
    }
  } else {
    const int lane = tid & 63, wid = tid >> 6;
    const int row = (blockIdx.x - 84) * 32 + wid * 8 + (lane >> 3);
    const int i8 = lane & 7;
    u32x4 v[7];
#pragma unroll
    for (int m = 0; m < 7; ++m) v[m] = *(const u32x4*)(x + (size_t)row * 448 + (i8 + 8 * m) * 8);
    float s = 0.f;
#pragma unroll
    for (int m = 0; m < 7; ++m)
#pragma unroll
      for (int j = 0; j < 4; ++j) s += bf_lo(v[m][j]) + bf_hi(v[m][j]);
    s += __shfl_xor(s, 1);
    s += __shfl_xor(s, 2);
    s += __shfl_xor(s, 4);
    const float mean = s * (1.f / 448.f);
    TS(1);
    float q = 0.f;
#pragma unroll
    for (int m = 0; m < 7; ++m)
#pragma unroll
      for (int j = 0; j < 4; ++j) {
        const float a = bf_lo(v[m][j]) - mean, b = bf_hi(v[m][j]) - mean;
        q += a * a + b * b;
      }
    q += __shfl_xor(q, 1);
    q += __shfl_xor(q, 2);
    q += __shfl_xor(q, 4);
    const float rstd = rsqrtf(q * (1.f / 448.f) + 1e-5f);
    char* dst = ws + WS_XN + (size_t)(row >> 4) * 14 * 1024 + (row & 15) * 16;
#pragma unroll
    for (int m = 0; m < 7; ++m) {
      u32x4 o;
#pragma unroll
      for (int j = 0; j < 4; ++j) o[j] = pk_f16((bf_lo(v[m][j]) - mean) * rstd, (bf_hi(v[m][j]) - mean) * rstd);
      const int ch = i8 + 8 * m;
      st128(dst + (ch >> 2) * 1024 + (ch & 3) * 256, o);
    }
  }
  TS(41); TFLUSH(0);
}

template <int OFF> DI void glds_o(const void* sbase, unsigned voff, unsigned laddr) {
  asm volatile("s_add_u32 m0, %2, %3\n\ts_nop 0\n\tglobal_load_lds_dwordx4 %0, %1" ::"v"(voff), "s"(sbase), "s"(laddr), "i"(OFF) : "scc");
}
template <int OFF> DI void glds_o48(const void* sbase, unsigned voff, unsigned laddr) {
  asm volatile("s_add_u32 m0, %2, %3\n\ts_mov_b32 exec_hi, 0xffff\n\tglobal_load_lds_dwordx4 %0, %1\n\ts_mov_b32 exec_hi, -1"
               ::"v"(voff), "s"(sbase), "s"(laddr), "i"(OFF) : "scc");
}

template <int RB, int NSEG, int NBS, int KS, int SK, int D, int LDW, bool PERM, bool SWAP>
struct Gemm {
  static constexpr int NST = KS / SK;
  static constexpr int NSLOT = D + 3;
  static constexpr int RS = 32 * NBS;
  static constexpr int PPK = 4 * RB + NSEG * NBS;
  static constexpr int STG = SK * PPK * 1024;
  static constexpr int LDS_BYTES = NSLOT * STG;
  static constexpr int NBI = NBS == 7 ? 2 : 1;
  static constexpr int CNT = SK * (RB + NSEG * NBI);
  static_assert(D + 1 < NST && D * CNT < 64 && (SK == 1 || SK == 2) && KS % 2 == 0, "cfg");

  template <class HK>
  static DI void load(int wl, unsigned lds0, const char* Ablk, const u16* W0, const u16* W1, int n0, int lane, HK&& hk TARG) {
    unsigned va[SK][RB], vb[SK][NBI];
#pragma unroll
    for (int i = 0; i < NBI; ++i) {
      const int e = wl * (NBS * 16) + i * 64 + lane;
      int pos, col;
      if constexpr (NBS == 7) {
        pos = (e * 4682) >> 16;
        col = (e - pos * 14) * 8;
        pos &= 31;
      } else {
        pos = e >> 3;
        const int slot = e & 7;
        col = 16 * ((slot >> 1) ^ ((pos >> 1) & 3)) + 8 * (slot & 1);
      }
      const int kk = PERM ? (8 * ((pos >> 2) & 3) + 4 * (pos >> 4) + (pos & 3)) : pos;
#pragma unroll
      for (int k = 0; k < SK; ++k) vb[k][i] = LDW ? ((k * 32 + kk) * LDW + n0 + col) * 2 : k * NBS * 1024 + e * 16;
    }
#pragma unroll
    for (int k = 0; k < SK; ++k)
#pragma unroll
      for (int r = 0; r < RB; ++r) va[k][r] = ((wl * RB + r) * KS + k) * 1024 + lane * 16;
    const unsigned mA = lds0 + wl * RB * 1024, mB = lds0 + 4 * RB * 1024 + wl * NBS * 256;
    const char* pa = Ablk;
    const char* p0 = (const char*)W0;
    const char* p1 = (const char*)W1;
    unsigned so = 0;
    auto issue = [&]() {
      const unsigned a = mA + so, b = mB + so;
      sfor<0, SK>([&](auto kk) {
        constexpr int k = decltype(kk)::v;
        sfor<0, RB>([&](auto rr) { glds_o<(k * PPK + decltype(rr)::v) * 1024>(pa, va[k][decltype(rr)::v], a); });
        glds_o<k * PPK * 1024>(p0, vb[k][0], b);
        if constexpr (NBI == 2) glds_o48<k * PPK * 1024 + 1024>(p0, vb[k][1], b);
        if constexpr (NSEG == 2) {
          glds_o<(k * PPK + NBS) * 1024>(p1, vb[k][0], b);
          if constexpr (NBI == 2) glds_o48<(k * PPK + NBS) * 1024 + 1024>(p1, vb[k][1], b);
        }
      });
      pa += SK * 1024;
      p0 += LDW ? SK * 32 * LDW * 2 : SK * NBS * 1024;
      p1 += LDW ? SK * 32 * LDW * 2 : SK * NBS * 1024;
      so += STG;
      so = so == NSLOT * STG ? 0 : so;
    };
#pragma unroll 1
    for (int T = -(D + 1); T < NST - D - 1; ++T) {
      if (T >= 0) wait_vm_bar<D * CNT>();
      issue();
      hk(T);
    }
    TS(1);
    sfor<0, D + 1>([&](auto tt) { wait_vm_bar<(D - decltype(tt)::v) * CNT>(); });
    bar();
  }

  template <int NW, int WR, class XR, class XF>
  static DI void compute(const lds_char* lds, f32x4 (&acc)[WR][NSEG * NW], int wid, int ns, int lane, XR&& xr, XF&& xf TARG) {
    constexpr int NBP = NBS == 7 ? 1 : NW;
    constexpr int NG = NSEG * NW;
    const unsigned ap0 = lds_addr(lds) + wid * WR * 1024 + lane * 16;
    unsigned bp0[NBP];
    if constexpr (NBS == 7) {
      bp0[0] = lds_addr(lds) + 4 * RB * 1024 + (lane >> 2) * 224 + 8 * (lane & 3) + ns * 32;
    } else {
      const unsigned tb = (lane >> 2) * 128 + (((lane >> 3) & 3) << 5) + 8 * (lane & 3);
#pragma unroll
      for (int g = 0; g < NW; ++g) bp0[g] = lds_addr(lds) + 4 * RB * 1024 + (tb ^ ((ns + g) << 5));
    }
    unsigned ap = ap0, bp[NBP];
#pragma unroll
    for (int g = 0; g < NBP; ++g) bp[g] = bp0[g];
    u32x4 a[2][WR];
    u32x4 b[SK][NG];
    auto rdA = [&](auto hh, auto kk) {
#pragma unroll
      for (int rb = 0; rb < WR; ++rb)
        a[decltype(hh)::v][rb] = lds_rd128((const lds_char*)(uintptr_t)ap + (decltype(kk)::v * PPK + rb) * 1024);
    };
    auto rdB = [&](auto kk, auto gg) {
      constexpr int k = decltype(kk)::v, g = decltype(gg)::v;
      constexpr int off = (k * PPK + (g / NW) * NBS) * 1024;
      const lds_char* p;
      if constexpr (NBS == 7) p = (const lds_char*)(uintptr_t)bp[0] + off + (g % NW) * 32;
      else p = (const lds_char*)(uintptr_t)bp[g % NW] + off;
      const u32x2 b0 = lds_tr(p), b1 = lds_tr(p + 16 * RS);
      b[k][g] = u32x4{b0[0], b0[1], b1[0], b1[1]};
    };
#pragma unroll
    for (int rb = 0; rb < WR; ++rb)
#pragma unroll
      for (int g = 0; g < NG; ++g) asm volatile("" : "+v"(acc[rb][g]));
    TS(1);
    bar();
    TS(2); TC(50);
    sfor<0, SK>([&](auto kk) {
      rdA(kk, kk);
      if constexpr (SK == 1) xr(IC<2>{});
      sfor<0, NG>([&](auto gg) { rdB(kk, gg); });
    });
    __builtin_amdgcn_s_waitcnt(0xc07f);
    sfor<0, NG * WR>([&](auto gg) { xf(gg, IC<1>{}, a[0]); });
    unsigned so = 0;
#pragma unroll 1
    for (int it = 0; it < KS / 2; ++it) {
      sfor<0, 2>([&](auto hh) {
        constexpr int h = decltype(hh)::v;
        constexpr int kb = SK == 1 ? 0 : h;
        if constexpr (SK == 1 || h == 0) {
          so += STG;
          so = so == NSLOT * STG ? 0 : so;
          ap = ap0 + so;
#pragma unroll
          for (int g = 0; g < NBP; ++g) bp[g] = bp0[g] + so;
          bar();
        }
        if constexpr (SK == 1) {
          rdA(IC<h ^ 1>{}, IC<0>{});
          xr(hh);
        }
        sched_fence();
        sfor<0, NG>([&](auto gg) {
          constexpr int g = decltype(gg)::v;
          sfor<0, WR>([&](auto rr) {
            constexpr int rb = decltype(rr)::v;
            acc[rb][g] = SWAP ? mfma16(b[kb][g], a[h][rb], acc[rb][g]) : mfma16(a[h][rb], b[kb][g], acc[rb][g]);
            if constexpr (rb < WR - 1) {
              xf(IC<g * WR + rb>{}, hh, a[h ^ 1]);
              sched_fence();
            }
          });
          rdB(IC<kb>{}, gg);
          if constexpr (SK == 2 && g == NG - 1) rdA(hh, hh);
          xf(IC<g * WR + WR - 1>{}, hh, a[h ^ 1]);
          sched_fence();
        });
      });
    }
  }
};

struct NoX {
  template <class... A> DI void operator()(A&&...) const {}
};

constexpr int D1 = 4;
#define XF0 6
typedef Gemm<2, 1, 7, 14, 1, D1, 1344, true, true> G1S;

template <bool SWAP, int NW>
DI void k1_compute(const lds_char* lds, char* __restrict__ ws, int mt, int nt, int wid, int lane TARG) {
  constexpr int ns = 0;
  typedef Gemm<2, 1, 7, 14, 1, D1, 1344, true, SWAP> G;
  const lds_char* vec = lds + G::LDS_BYTES;
  f32x4 acc[2][NW];
#pragma unroll
  for (int rb = 0; rb < 2; ++rb)
#pragma unroll
    for (int nb = 0; nb < NW; ++nb) acc[rb][nb] = f32x4{0.f, 0.f, 0.f, 0.f};
  const int gq = lane >> 4, t = lane & 15;
  const lds_char* vp = vec + gq * 32;
  f32x4 sv[2], mv[2];
  G::template compute<NW, 2>(
      lds, acc, wid, ns, lane,
      [&](auto hh) {
        if constexpr (decltype(hh)::v == 2) {
          sv[0] = lds_rd128f(vp);
          sv[1] = lds_rd128f(vp + 16);
          mv[0] = lds_rd128f(vp + 1792);
          mv[1] = lds_rd128f(vp + 1792 + 16);
          vp += 128;
        }
      },
      [&](auto gg, auto, u32x4(&a)[2]) {
        constexpr int g = decltype(gg)::v - XF0;
        if constexpr (g >= 0 && g < 8) {
          constexpr int j = g >> 1, rb = g & 1;
          a[rb][j] = pk_bf16(mix_lo(a[rb][j], mv[j >> 1][(j & 1) * 2], sv[j >> 1][(j & 1) * 2]),
                             mix_hi(a[rb][j], mv[j >> 1][(j & 1) * 2 + 1], sv[j >> 1][(j & 1) * 2 + 1]));
          if constexpr ((g & 3) == 3) {
            sv[g >> 2] = lds_rd128f(vp + (g >> 2) * 16);
            mv[g >> 2] = lds_rd128f(vp + 1792 + (g >> 2) * 16);
            if constexpr (g == 7) vp += 128;
          }
        }
      } TPASS);
  TS(40); TC(51);
  const int sec = nt >> 2, h0 = (nt & 3) * 4;
  if constexpr (SWAP) {
    const float sc = sec == 0 ? 0.18898223650461362f * 1.4426950408889634f : 1.f;
    char* base = ws + (sec == 0 ? WS_Q : WS_K);
#pragma unroll
    for (int rb = 0; rb < 2; ++rb) {
      const int tb = mt * 8 + wid * 2 + rb;
#pragma unroll
      for (int nb = 0; nb < NW; ++nb) {
        const int Gi = 4 * (ns + nb) + gq;
        const int hd = (Gi * 37) >> 8, dg = Gi - 7 * hd;
        char* dst = base + ((size_t)(h0 + hd) * 128 + tb) * 1024 + ((dg >> 1) * 16 + t) * 16 + (dg & 1) * 8;
        const f32x4 v = acc[rb][nb];
        const unsigned lo = pk_bf16(v[0] * sc, v[1] * sc), hi = pk_bf16(v[2] * sc, v[3] * sc);
        if (dg == 6) st128(dst, u32x4{lo, hi, 0u, 0u});
        else st64(dst, u32x2{lo, hi});
      }
    }
  } else {
    const int s32 = mt * 4 + wid;
    char* base = ws + WS_V;
#pragma unroll
    for (int nb = 0; nb < NW; ++nb) {
      const int n = 16 * (ns + nb) + t;
      const int hd = (n * 147) >> 12, d = n - 28 * hd;
      char* dst = base + (((size_t)(h0 + hd) * 64 + s32) * 2 + (d >> 4)) * 1024 + (gq * 16 + (d & 15)) * 16;
      const f32x4 v0 = acc[0][nb], v1 = acc[1][nb];
      st128(dst, u32x4{pk_bf16(v0[0], v0[1]), pk_bf16(v0[2], v0[3]), pk_bf16(v1[0], v1[1]), pk_bf16(v1[2], v1[3])});
    }
    if (lane < 16) {
      char* dst = base + (((size_t)(h0 + (lane >> 2)) * 64 + s32) * 2 + 1) * 1024 + ((lane & 3) * 16 + 12) * 16;
      st128(dst, u32x4{0x3f803f80u, 0x3f803f80u, 0x3f803f80u, 0x3f803f80u});
    }
  }
}

extern "C" __global__ __launch_bounds__(512, 1) void k1_qkv(const u16* __restrict__ wqkv, const u16* __restrict__ wg,
                                                 const u16* __restrict__ wu, const u16* __restrict__ wo,
    const u16* __restrict__ wd, char* __restrict__ ws) {
  TDECL; TS(0);
  extern __shared__ __attribute__((aligned(16))) char smem[];
  lds_char* lds = (lds_char*)smem;
  const int lane = threadIdx.x & 63, wid = __builtin_amdgcn_readfirstlane(threadIdx.x >> 6);
  const int bid = blockIdx.x, xcd = bid & 7, j = bid >> 3;
  if (j >= 24) {
    const int e = (j - 24) * 8 + xcd;
    const int mat = e >> 5, c0 = (e & 31) * 56;
    const u16* W = mat ? wu : wg;
    const int tid = threadIdx.x;
    float* red = (float*)smem;
    if (tid < 448) {
      const int rg = tid / 7, cq = tid - rg * 7;
      u32x4 w[7];
      float m[7], sb[7];
#pragma unroll
      for (int i = 0; i < 7; ++i) {
        const int k = rg + 64 * i;
        w[i] = *(const u32x4*)(W + (size_t)k * 1792 + c0 + cq * 8);
        sb[i] = ((const float*)(ws + WS_MOD))[3 * 448 + k];
        m[i] = ((const float*)(ws + WS_MOD))[4 * 448 + k];
      }
      float a1[8], a2[8];
#pragma unroll
      for (int q = 0; q < 8; ++q) a1[q] = a2[q] = 0.f;
#pragma unroll
      for (int i = 0; i < 7; ++i)
#pragma unroll
        for (int q = 0; q < 4; ++q) {
          a1[2 * q] += m[i] * bf_lo(w[i][q]);
          a1[2 * q + 1] += m[i] * bf_hi(w[i][q]);
          a2[2 * q] += sb[i] * bf_lo(w[i][q]);
          a2[2 * q + 1] += sb[i] * bf_hi(w[i][q]);
        }
#pragma unroll
      for (int q = 0; q < 8; ++q) {
        red[rg * 113 + cq * 8 + q] = a1[q];
        red[rg * 113 + 56 + cq * 8 + q] = a2[q];
      }
    }
    __syncthreads();
    if (tid < 448) {
      const int col = tid >> 2, part = tid & 3;
      float s = 0.f;
#pragma unroll
      for (int i = 0; i < 16; ++i) s += red[(part * 16 + i) * 113 + col];
      s += __shfl_xor(s, 1);
      s += __shfl_xor(s, 2);
      if (part == 0) {
        const int which = col >= 56, n = c0 + col - which * 56;
        const int nt = n / 112;
        ((float*)(ws + WS_U))[(nt * 4 + mat * 2 + which) * 112 + n - nt * 112] = s;
      }
    }
    u32x4 tw = {0u, 0u, 0u, 0u};
#pragma unroll
    for (int i = 0; i < 4; ++i) {
      const int ch = e + 64 * i;
      if (ch < 245) tw ^= *(const u32x4*)((ch < 49 ? (const char*)wo + (size_t)ch * 8192 : (const char*)wd + (size_t)(ch - 49) * 8192) + tid * 16);
    }
    asm volatile("" ::"v"(tw));
    TS(41); TFLUSH(1);
    return;
  }
  const int mt = (xcd >> 1) * 4 + (j & 3), nt = (xcd & 1) * 6 + (j >> 2);
  if (wid < 4) {
    const int off = wid * 1024 + lane * 16;
    glds(ws + WS_MOD, off < 3584 ? off : 0, lds_addr(lds) + G1S::LDS_BYTES + wid * 1024);
    G1S::load(wid, lds_addr(lds), ws + WS_XN + (size_t)mt * 8 * 14 * 1024, wqkv, wqkv, nt * 112, lane, NoX{} TPASS);
  } else {
    if (nt < 8) k1_compute<true, 7>(lds, ws, mt, nt, wid - 4, lane TPASS);
    else k1_compute<false, 7>(lds, ws, mt, nt, wid - 4, lane TPASS);
  }
  TS(41); TFLUSH(1);
}

DI void k2_wave(const lds_char* lds, char* __restrict__ ws, int qt, int head, int wid, int lane) {
  const int gq = lane >> 4, t = lane & 15;
  const int lp0 = wid >> 1;
  const int qb = 8 * qt + wid;
  const lds_char* kp = lds + lp0 * 2048 + lane * 16;
  const lds_char* vp = lds + 24576 + lp0 * 2048 + lane * 16;
  const u32x4 qf = lds_rd128(lds + 49152 + wid * 1024 + lane * 16);
  f32x4 sc[18];
  const float NEG = -__builtin_inff();
#pragma unroll
  for (int i = 0; i < 18; ++i) {
    const u32x4 kf = lds_rd128(kp + i * 1024);
    sc[i] = mfma16(kf, qf, f32x4{0.f, 0.f, 0.f, 0.f});
  }
  const int d0 = 256 + 16 * (wid & 1) + t - 4 * gq;
#pragma unroll
  for (int r = 0; r < 4; ++r) {
    if (d0 - r >= 256) sc[0][r] = NEG;
    if (d0 - 16 - r >= 256) sc[1][r] = NEG;
    if (d0 - 256 - r < 0) sc[16][r] = NEG;
    if (d0 - 272 - r < 0) sc[17][r] = NEG;
  }
  if (qt < 2) {
    const int kb0 = 8 * qt - 16 + 2 * lp0;
#pragma unroll
    for (int i = 0; i < 16; ++i)
      if (kb0 + i < 0) sc[i] = f32x4{NEG, NEG, NEG, NEG};
  }
  float m = sc[16][0];
#pragma unroll
  for (int i = 0; i < 18; ++i) m = fmaxf(fmaxf(m, fmaxf(sc[i][0], sc[i][1])), fmaxf(sc[i][2], sc[i][3]));
  m = fmaxf(m, __shfl_xor(m, 16));
  m = fmaxf(m, __shfl_xor(m, 32));
  unsigned pp[18][2];
#pragma unroll
  for (int i = 0; i < 18; ++i) {
    pp[i][0] = pk_bf16(__builtin_amdgcn_exp2f(sc[i][0] - m), __builtin_amdgcn_exp2f(sc[i][1] - m));
    pp[i][1] = pk_bf16(__builtin_amdgcn_exp2f(sc[i][2] - m), __builtin_amdgcn_exp2f(sc[i][3] - m));
  }
  wait_vm_bar<0>();
  f32x4 o0 = {0.f, 0.f, 0.f, 0.f}, o1 = {0.f, 0.f, 0.f, 0.f};
#pragma unroll
  for (int p = 0; p < 9; ++p) {
    const u32x4 pf = {pp[2 * p][0], pp[2 * p][1], pp[2 * p + 1][0], pp[2 * p + 1][1]};
    const u32x4 v0 = lds_rd128(vp + p * 2048), v1 = lds_rd128(vp + p * 2048 + 1024);
    o0 = mfma16(v0, pf, o0);
    o1 = mfma16(v1, pf, o1);
  }
  const float sum = __shfl(o1[0], 48 + t);
  const float inv = __builtin_amdgcn_rcpf(sum);
  char* base = ws + WS_O + (size_t)qb * 14 * 1024;
  {
    const int Gi = 7 * head + gq;
    st64(base + (Gi >> 3) * 1024 + (((Gi >> 1) & 3) * 16 + t) * 16 + (Gi & 1) * 8, u32x2{pk_bf16(o0[0] * inv, o0[1] * inv), pk_bf16(o0[2] * inv, o0[3] * inv)});
  }
  if (gq < 3) {
    const int Gi = 7 * head + 4 + gq;
    st64(base + (Gi >> 3) * 1024 + (((Gi >> 1) & 3) * 16 + t) * 16 + (Gi & 1) * 8, u32x2{pk_bf16(o1[0] * inv, o1[1] * inv), pk_bf16(o1[2] * inv, o1[3] * inv)});
  }
}

extern "C" __global__ __launch_bounds__(512, 2) void k2_attn(char* __restrict__ ws) {
  TDECL; TS(0);
  extern __shared__ __attribute__((aligned(16))) char smem[];
  lds_char* lds = (lds_char*)smem;
  const int lane = threadIdx.x & 63, wid = __builtin_amdgcn_readfirstlane(threadIdx.x >> 6);
  const int bid = blockIdx.x, xcd = bid & 7, j = bid >> 3;
  const int head = xcd * 2 + (j & 1), qt = j >> 1;
  const unsigned l0 = lds_addr(lds);
  glds(ws + WS_Q + ((size_t)head * 128 + 8 * qt + wid) * 1024, lane * 16, l0 + 49152 + wid * 1024);
#pragma unroll
  for (int i = 0; i < 3; ++i) {
    const int lk = wid * 3 + i;
    int tb = 8 * qt - 16 + lk;
    tb = tb < 0 ? 0 : tb;
    glds(ws + WS_K + ((size_t)head * 128 + tb) * 1024, lane * 16, l0 + lk * 1024);
  }
#pragma unroll
  for (int i = 0; i < 3; ++i) {
    const int lv = wid * 3 + i;
    int f = (4 * qt - 8) * 2 + lv;
    f = f < 0 ? (lv & 1) : f;
    glds(ws + WS_V + ((size_t)head * 128 + f) * 1024, lane * 16, l0 + 24576 + lv * 1024);
  }
  TS(1);
  wait_vm_bar<3>();
  TS(2);
  k2_wave(lds, ws, qt, head, wid, lane);
  TS(41); TFLUSH(2);
}

typedef Gemm<1, 1, 4, 14, 2, 4, 448, true, true> G3;
extern "C" __global__ __launch_bounds__(512, 1) void k3_wo(const u16* __restrict__ x, const u16* __restrict__ wo, char* __restrict__ ws) {
  TDECL; TS(0);
  extern __shared__ __attribute__((aligned(16))) char smem[];
  lds_char* lds = (lds_char*)smem;
  const int lane = threadIdx.x & 63, wid = __builtin_amdgcn_readfirstlane(threadIdx.x >> 6);
  const int bid = blockIdx.x, xcd = bid & 7, j = bid >> 3;
  const int mt = xcd * 4 + (j & 3), nt = j >> 2;
  if (wid >= 4) {
    G3::load(wid - 4, lds_addr(lds), ws + WS_O + (size_t)mt * 4 * 14 * 1024, wo, wo, nt * 64, lane, NoX{} TPASS);
    TS(41); TFLUSH(3);
    return;
  }
  const int gq = lane >> 4, t = lane & 15;
  const int tb = mt * 4 + wid, row = tb * 16 + t;
  u32x2 xr[4];
  f32x4 ga[4], m2[4];
#pragma unroll
  for (int nb = 0; nb < 4; ++nb) {
    xr[nb] = *(const u32x2*)(x + (size_t)row * 448 + nt * 64 + nb * 16 + gq * 4);
    ga[nb] = *(const f32x4*)((const float*)(ws + WS_MOD) + 2 * 448 + nt * 64 + nb * 16 + gq * 4);
    m2[nb] = *(const f32x4*)((const float*)(ws + WS_MOD) + 4 * 448 + nt * 64 + nb * 16 + gq * 4);
  }
  f32x4 acc[1][4];
#pragma unroll
  for (int nb = 0; nb < 4; ++nb) acc[0][nb] = f32x4{0.f, 0.f, 0.f, 0.f};
  G3::compute<4, 1>(lds, acc, wid, 0, lane, NoX{}, NoX{} TPASS);
  TS(40); TC(51);
  unsigned o[4][2], om[4][2];
  float v[4][4];
  float s = 0.f;
#pragma unroll
  for (int nb = 0; nb < 4; ++nb) {
    o[nb][0] = pk_bf16(bf_lo(xr[nb][0]) + ga[nb][0] * acc[0][nb][0], bf_hi(xr[nb][0]) + ga[nb][1] * acc[0][nb][1]);
    o[nb][1] = pk_bf16(bf_lo(xr[nb][1]) + ga[nb][2] * acc[0][nb][2], bf_hi(xr[nb][1]) + ga[nb][3] * acc[0][nb][3]);
    v[nb][0] = bf_lo(o[nb][0]); v[nb][1] = bf_hi(o[nb][0]); v[nb][2] = bf_lo(o[nb][1]); v[nb][3] = bf_hi(o[nb][1]);
    om[nb][0] = pk_bf16(v[nb][0] * m2[nb][0], v[nb][1] * m2[nb][1]);
    om[nb][1] = pk_bf16(v[nb][2] * m2[nb][2], v[nb][3] * m2[nb][3]);
    s += (v[nb][0] + v[nb][1]) + (v[nb][2] + v[nb][3]);
  }
  char* dst = ws + WS_X1 + ((size_t)tb * 14 + nt * 2) * 1024 + lane * 16;
  st128(dst + (WS_XM - WS_X1), u32x4{om[0][0], om[0][1], om[1][0], om[1][1]});
  st128(dst + (WS_XM - WS_X1) + 1024, u32x4{om[2][0], om[2][1], om[3][0], om[3][1]});
  st128(dst, u32x4{o[0][0], o[0][1], o[1][0], o[1][1]});
  st128(dst + 1024, u32x4{o[2][0], o[2][1], o[3][0], o[3][1]});
  s += __shfl_xor(s, 16);
  s += __shfl_xor(s, 32);
  const float mc = s * (1.f / 64.f);
  float q = 0.f;
#pragma unroll
  for (int nb = 0; nb < 4; ++nb)
#pragma unroll
    for (int r = 0; r < 4; ++r) q += (v[nb][r] - mc) * (v[nb][r] - mc);
  q += __shfl_xor(q, 16);
  q += __shfl_xor(q, 32);
  if (gq == 0) *(f32x2*)(ws + WS_ST + ((size_t)row * 7 + nt) * 8) = f32x2{mc, q};
  TS(41); TFLUSH(3);
}

typedef Gemm<2, 2, 7, 14, 1, 3, 1792, false, true> G4;
template <int NW>
DI void k4_compute(const lds_char* lds, char* __restrict__ ws, int mt, int nt, int wid, int lane TARG) {
  const int odd = nt & 1;
  const int ns = NW == 4 ? 3 * odd : 4 - 4 * odd;
  const int gq = lane >> 4, t = lane & 15;
  f32x4 acc[2][2 * NW];
#pragma unroll
  for (int rb = 0; rb < 2; ++rb)
#pragma unroll
    for (int nb = 0; nb < 2 * NW; ++nb) acc[rb][nb] = f32x4{0.f, 0.f, 0.f, 0.f};
  G4::compute<NW, 2>(lds, acc, wid, ns, lane, NoX{}, NoX{} TPASS);
  TS(40); TC(51);
  const lds_char* ex = lds + G4::LDS_BYTES;
  const lds_char* up = ex + gq * 16 + ns * 64;
  const int B0 = nt * 7 + ns;
  typedef const __attribute__((address_space(3))) f32x2* lp2;
  const f32x2 st0 = *(lp2)(ex + 9216 + (wid * 32 + t) * 8), st1 = *(lp2)(ex + 9216 + (wid * 32 + 16 + t) * 8);
  char* base = ws + WS_HH + (size_t)(mt * 8 + wid * 2) * 56 * 1024 + lane * 16 + (B0 >> 1) * 1024;
  unsigned o[2][NW][2];
#pragma unroll
  for (int nb = 0; nb < NW; ++nb) {
    const f32x4 u1g = lds_rd128f(up + nb * 64), u2g = lds_rd128f(up + nb * 64 + 448);
    const f32x4 u1u = lds_rd128f(up + nb * 64 + 896), u2u = lds_rd128f(up + nb * 64 + 1344);
#pragma unroll
    for (int rb = 0; rb < 2; ++rb) {
      const f32x2 st = rb ? st1 : st0;
      const f32x2 nm = {st[0], st[0]}, rs2 = {st[1], st[1]};
#pragma unroll
      for (int r = 0; r < 2; ++r) {
        const f32x2 ag = {acc[rb][nb][2 * r], acc[rb][nb][2 * r + 1]}, au = {acc[rb][NW + nb][2 * r], acc[rb][NW + nb][2 * r + 1]};
        const f32x2 gv = rs2 * ag + (nm * f32x2{u1g[2 * r], u1g[2 * r + 1]} + f32x2{u2g[2 * r], u2g[2 * r + 1]});
        const f32x2 uv = rs2 * au + (nm * f32x2{u1u[2 * r], u1u[2 * r + 1]} + f32x2{u2u[2 * r], u2u[2 * r + 1]});
        const f32x2 z = gv * (gv * gv * f32x2{-0.1029432397f, -0.1029432397f} + f32x2{-2.302208198f, -2.302208198f});
        const f32x2 d = f32x2{__builtin_amdgcn_exp2f(z[0]), __builtin_amdgcn_exp2f(z[1])} + f32x2{1.f, 1.f};
        const f32x2 h = gv * uv * f32x2{__builtin_amdgcn_rcpf(d[0]), __builtin_amdgcn_rcpf(d[1])};
        o[rb][nb][r] = pk_bf16(h[0], h[1]);
      }
      if (NW == 4 && (nb & 1))
        st128(base + rb * 56 * 1024 + (nb >> 1) * 1024, u32x4{o[rb][nb - 1][0], o[rb][nb - 1][1], o[rb][nb][0], o[rb][nb][1]});
    }
  }
  if constexpr (NW == 3) {
#pragma unroll
    for (int rb = 0; rb < 2; ++rb) {
      char* bs = base + rb * 56 * 1024;
      if (odd) {
        st64(bs + 8, u32x2{o[rb][0][0], o[rb][0][1]});
        st128(bs + 1024, u32x4{o[rb][1][0], o[rb][1][1], o[rb][2][0], o[rb][2][1]});
      } else {
        st128(bs, u32x4{o[rb][0][0], o[rb][0][1], o[rb][1][0], o[rb][1][1]});
        st64(bs + 1024, u32x2{o[rb][2][0], o[rb][2][1]});
      }
    }
  }
}

extern "C" __global__ __launch_bounds__(768, 3) void k4_gateup(const u16* __restrict__ wg, const u16* __restrict__ wu, char* __restrict__ ws) {
  TDECL; TS(0);
  extern __shared__ __attribute__((aligned(16))) char smem[];
  lds_char* lds = (lds_char*)smem;
  const int lane = threadIdx.x & 63, wid = __builtin_amdgcn_readfirstlane(threadIdx.x >> 6);
  const int bid = blockIdx.x, xcd = bid & 7, j = bid >> 3;
  const int mt = (xcd >> 2) * 8 + (j & 7), nt = (xcd & 3) * 4 + (j >> 3);
  if (wid < 4) {
    const int wl = wid;
    const unsigned ex = lds_addr(lds) + G4::LDS_BYTES;
    if (wl < 2) {
      const int off = wl * 1024 + lane * 16;
      glds(ws + WS_U + (size_t)nt * 1792, off < 1792 ? off : 0, ex + wl * 1024);
    }
#pragma unroll
    for (int i = 0; i < 2; ++i) {
      const int o2 = (wl * 2 + i) * 1024 + lane * 16;
      glds(ws + WS_ST + (size_t)mt * 7168, o2 < 7168 ? o2 : 0, ex + 2048 + (wl * 2 + i) * 1024);
    }
    float mc[7], q = 0.f, mean = 0.f;
#pragma unroll
    for (int c = 0; c < 7; ++c) mc[c] = 0.f;
    const int row = wl * 32 + (lane & 31);
    G4::load(wl, lds_addr(lds), ws + WS_XM + (size_t)mt * 8 * 14 * 1024, wg, wu, nt * 112, lane, [&](int T) {
      if (T == 1) {
        const lds_char* sp = lds + G4::LDS_BYTES + 2048 + row * 56;
        float sm = 0.f;
#pragma unroll
        for (int c = 0; c < 7; ++c) {
          const f32x2 e = *(const __attribute__((address_space(3))) f32x2*)(sp + c * 8);
          mc[c] = e[0]; q += e[1]; sm += e[0];
        }
        mean = sm * (1.f / 7.f);
      }
      if (T == 3) {
#pragma unroll
        for (int c = 0; c < 7; ++c) q += 64.f * (mc[c] - mean) * (mc[c] - mean);
        const float rs = rsqrtf(q * (1.f / 448.f) + 1e-5f);
        *(__attribute__((address_space(3))) f32x2*)(lds + G4::LDS_BYTES + 9216 + row * 8) = f32x2{-mean * rs, rs};
        asm volatile("s_waitcnt lgkmcnt(0)" ::: "memory");
      }
    } TPASS);
  } else if (wid < 8) {
    k4_compute<4>(lds, ws, mt, nt, wid - 4, lane TPASS);
  } else {
    k4_compute<3>(lds, ws, mt, nt, wid - 8, lane TPASS);
  }
  TS(41); TFLUSH(4);
}

typedef Gemm<1, 1, 4, 56, 2, 4, 448, false, true> G5;
extern "C" __global__ __launch_bounds__(512, 1) void k5_down(const u16* __restrict__ wd, char* __restrict__ ws, u16* __restrict__ out) {
  TDECL; TS(0);
  extern __shared__ __attribute__((aligned(16))) char smem[];
  lds_char* lds = (lds_char*)smem;
  const int lane = threadIdx.x & 63, wid = __builtin_amdgcn_readfirstlane(threadIdx.x >> 6);
  const int bid = blockIdx.x, xcd = bid & 7, j = bid >> 3;
  const int mt = xcd * 4 + (j & 3), nt = j >> 2;
  if (wid >= 4) {
    G5::load(wid - 4, lds_addr(lds), ws + WS_HH + (size_t)mt * 4 * 56 * 1024, wd, wd, nt * 64, lane, NoX{} TPASS);
    TS(41); TFLUSH(5);
    return;
  }
  const int gq = lane >> 4, t = lane & 15, wr = wid >> 1, wc = wid & 1;
  const int tb = mt * 4 + wr * 2;
  u32x4 xr[2];
  f32x4 gb[2];
#pragma unroll
  for (int i = 0; i < 2; ++i) xr[i] = *(const u32x4*)(ws + WS_X1 + ((size_t)(tb + i) * 14 + nt * 2 + wc) * 1024 + lane * 16);
#pragma unroll
  for (int nb = 0; nb < 2; ++nb) gb[nb] = *(const f32x4*)((const float*)(ws + WS_MOD) + 5 * 448 + nt * 64 + wc * 32 + nb * 16 + gq * 4);
  f32x4 acc[2][2];
#pragma unroll
  for (int nb = 0; nb < 2; ++nb) acc[0][nb] = acc[1][nb] = f32x4{0.f, 0.f, 0.f, 0.f};
  G5::compute<2, 2>(lds, acc, wr, wc * 2, lane, NoX{}, NoX{} TPASS);
  TS(40); TC(51);
#pragma unroll
  for (int rb = 0; rb < 2; ++rb) {
    unsigned lo[2], hi[2];
#pragma unroll
    for (int nb = 0; nb < 2; ++nb) {
      const unsigned x0 = xr[rb][nb * 2], x1 = xr[rb][nb * 2 + 1];
      lo[nb] = pk_bf16(bf_lo(x0) + gb[nb][0] * acc[rb][nb][0], bf_hi(x0) + gb[nb][1] * acc[rb][nb][1]);
      hi[nb] = pk_bf16(bf_lo(x1) + gb[nb][2] * acc[rb][nb][2], bf_hi(x1) + gb[nb][3] * acc[rb][nb][3]);
    }
    const auto l = __builtin_amdgcn_permlane16_swap(lo[0], lo[1], false, false);
    const auto h = __builtin_amdgcn_permlane16_swap(hi[0], hi[1], false, false);
    u16* dst = out + (size_t)((tb + rb) * 16 + t) * 448 + nt * 64 + wc * 32 + (gq & 1) * 16 + (gq >> 1) * 8;
    st128(dst, u32x4{l[0], h[0], l[1], h[1]});
  }
  TS(41); TFLUSH(5);
}

extern "C" void kernel_launch(void* const* d_in, const int* in_sizes, int n_in, void* d_out, int out_size, void* d_ws,
                              size_t ws_size, hipStream_t stream) {
  const u16* x = (const u16*)d_in[0];
  const u16* wqkv = (const u16*)d_in[1];
  const u16* wo = (const u16*)d_in[2];
  const u16* wg = (const u16*)d_in[4];
  const u16* wu = (const u16*)d_in[5];
  const u16* wd = (const u16*)d_in[6];
  const u16* c = (const u16*)d_in[7];
  const u16* wmod = (const u16*)d_in[8];
  char* ws = (char*)d_ws;
  constexpr int L1 = G1S::LDS_BYTES + 4096;
  constexpr int L2 = 49152 + 8192;
  constexpr int L3 = G3::LDS_BYTES;
  constexpr int L4 = G4::LDS_BYTES + 2048 + 8192;
  constexpr int L5 = G5::LDS_BYTES;
  static_assert(L1 <= 163840 && L3 <= 163840 && L4 <= 163840 && L5 <= 163840, "lds");
  (void)hipFuncSetAttribute((const void*)k1_qkv, hipFuncAttributeMaxDynamicSharedMemorySize, L1);
  (void)hipFuncSetAttribute((const void*)k3_wo, hipFuncAttributeMaxDynamicSharedMemorySize, L3);
  (void)hipFuncSetAttribute((const void*)k4_gateup, hipFuncAttributeMaxDynamicSharedMemorySize, L4);
  (void)hipFuncSetAttribute((const void*)k5_down, hipFuncAttributeMaxDynamicSharedMemorySize, L5);
  k0_mod_ln<<<256, 256, 0, stream>>>(x, c, wmod, wqkv, wo, wg, wu, wd, (const u16*)d_out, ws);
  k1_qkv<<<256, 512, L1, stream>>>(wqkv, wg, wu, wo, wd, ws);
  k2_attn<<<256, 512, L2, stream>>>(ws);
  k3_wo<<<224, 512, L3, stream>>>(x, wo, ws);
  k4_gateup<<<256, 768, L4, stream>>>(wg, wu, ws);
  k5_down<<<224, 512, L5, stream>>>(wd, ws, (u16*)d_out);
}
```
